```python
import torch

def _softmax(x, axis=-1):
    return torch.softmax(x, dim=axis)
def _gelu(x):
    return torch.nn.functional.gelu(x, approximate="tanh")
def _silu(x):
    return torch.nn.functional.silu(x)
def _sigmoid(x):
    return torch.sigmoid(x)
def _layernorm(x, axis=-1, eps=1e-5):
    m = x.mean(dim=axis, keepdim=True)
    v = x.var(dim=axis, keepdim=True, unbiased=False)
    return (x - m) / torch.sqrt(v + eps)
def _scatter_add(tgt, idx, src, axis=0):
    return tgt.clone().index_add_(axis, idx, src)
def _segment_sum(src, seg, n):
    out = torch.zeros((n,) + src.shape[1:], dtype=src.dtype)
    return out.index_add_(0, seg, src)
def _pad(x, pw):
    flat = tuple(v for pair in reversed(pw) for v in pair)
    return torch.nn.functional.pad(x, flat)

kernel_name = 'parallel_sum__gqa_block_cached__dense_activation_gelu__decode'

def setup_inputs():

    _w = (lambda a: (lambda t: t.to(torch.bfloat16) if t.is_floating_point() else t)(torch.as_tensor(a)))
    def _A_setup():
        B, Sq, D = 2, 1, 512
        Hq, Hkv, Sk, Dh = 32, 8, 8192, 128
        assert Hq % Hkv == 0, (Hq, Hkv)
        return {
            "x": _w((lambda *s: torch.randn(*s, dtype=torch.bfloat16) * 0.05)(B, Sq, D)),
            "k_cache": _w((lambda *s: torch.randn(*s, dtype=torch.bfloat16) * 1.6)(B, Hkv, Sk, Dh)),
            "v_cache": _w((lambda *s: torch.randn(*s, dtype=torch.bfloat16) * 0.8)(B, Hkv, Sk, Dh)),
            "wq": _w((lambda *s: torch.randn(*s, dtype=torch.bfloat16) * 1.6)(D, Hq * Dh) * (D ** -0.5)),
            "wo": _w((lambda *s: torch.randn(*s, dtype=torch.bfloat16) * 0.8)(Hq * Dh, D) * ((Hq * Dh) ** -0.5)),
        }
    _A_in = _A_setup()

    def _B_setup():
        B, S, D, F = 2, 1, 512, 1792
        return {
            "x": _w((lambda *s: torch.randn(*s, dtype=torch.bfloat16) * 0.05)(B, S, D)),
            "w1": _w((lambda *s: torch.randn(*s, dtype=torch.bfloat16) * 0.4)(D, F) * (D ** -0.5)),
            "b1": _w((lambda *s: torch.randn(*s, dtype=torch.bfloat16) * 0.4)(F) * 0.01),
            "w2": _w((lambda *s: torch.randn(*s, dtype=torch.bfloat16) * 0.4)(F, D) * (F ** -0.5)),
            "b2": _w((lambda *s: torch.randn(*s, dtype=torch.bfloat16) * 0.4)(D) * 0.01),
        }
    _B_in = _B_setup()

    return {
        "A_x": _A_in["x"],
        "A_k_cache": _A_in["k_cache"],
        "A_v_cache": _A_in["v_cache"],
        "A_wq": _A_in["wq"],
        "A_wo": _A_in["wo"],
        "B_x": _B_in["x"],
        "B_w1": _B_in["w1"],
        "B_b1": _B_in["b1"],
        "B_w2": _B_in["w2"],
        "B_b2": _B_in["b2"],
    }

def reference(A_x, A_k_cache, A_v_cache, A_wq, A_wo, B_x, B_w1, B_b1, B_w2, B_b2):
    def _A_ref(x, k_cache, v_cache, wq, wo):
        B, Sq, D = x.shape
        Hq, Dh = 32, 128
        Hkv = k_cache.shape[1]
        rep = Hq // Hkv
        q = (lambda x, *d: x.permute(*d))((x @ wq).reshape(B, Sq, Hq, Dh), 0, 2, 1, 3)
        k = (lambda x, n, axis: torch.repeat_interleave(x, n, dim=axis))(k_cache, rep, axis=1)
        v = (lambda x, n, axis: torch.repeat_interleave(x, n, dim=axis))(v_cache, rep, axis=1)
        scores = torch.einsum("bhqd,bhkd->bhqk", q, k) * (Dh ** -0.5)
        p = _softmax(scores, axis=-1)
        o = torch.einsum("bhqk,bhkd->bhqd", p, v)
        return (lambda x, *d: x.permute(*d))(o, 0, 2, 1, 3).reshape(B, Sq, Hq * Dh) @ wo

    def _B_ref(x, w1, b1, w2, b2):
        h = torch.einsum("bsd,df->bsf", x, w1) + b1
        h = _gelu(h)
        return torch.einsum("bsf,fd->bsd", h, w2) + b2

    x = A_x
    xn = _layernorm(x, axis=-1)
    return x + _A_ref(xn, A_k_cache, A_v_cache, A_wq, A_wo) + _B_ref(xn, B_w1, B_b1, B_w2, B_b2)
```

```cpp
#include <hip/hip_runtime.h>
#include <hip/hip_bf16.h>
#include <stdint.h>

#define GAS __attribute__((address_space(1)))
#define LAS __attribute__((address_space(3)))
typedef unsigned u32x4 __attribute__((ext_vector_type(4)));
typedef unsigned u32x2 __attribute__((ext_vector_type(2)));
typedef float f32x4 __attribute__((ext_vector_type(4)));
typedef float f32x2 __attribute__((ext_vector_type(2)));
typedef short s16x4 __attribute__((ext_vector_type(4)));
typedef __bf16 bf16x8 __attribute__((ext_vector_type(8)));
typedef unsigned short u16;
typedef unsigned long long u64;

#define KTRIG 8
#define QSLEEP 48
#define PDEPTH 1
#define INITV 2
#define PSLEEP 4

constexpr int DM = 512, HQ = 32, HKV = 8, SK = 8192, DH = 128, FF = 1792;
constexpr int KEYS = 512;
constexpr int NAUX = 6, NSTR = 8, NTHR = (NAUX + NSTR) * 64;
constexpr float SCALE = 0.08838834764831845f, LOG2E = 1.4426950408889634f;
constexpr unsigned SPIN_CAP = 1u << 17;
constexpr unsigned AUX_CAP = SPIN_CAP;
#define SPIN_SLEEP 1

constexpr unsigned WS_EPOCH = 0;
constexpr unsigned WS_ML = 16384;
constexpr unsigned WS_Q = 32768;
constexpr unsigned WS_HP = WS_Q + 131072;
constexpr unsigned WS_H1 = 524288;
constexpr unsigned WS_H2 = WS_H1 + (1u << 20);
constexpr unsigned WS_END = WS_H2 + (1u << 20);
constexpr unsigned WS_STAMP = 8u << 20;

constexpr unsigned L_V = 0;
constexpr unsigned L_WST = 131072;
constexpr unsigned L_XN = L_WST + 24576;
constexpr unsigned L_P = L_XN;
constexpr unsigned L_Q = L_XN + 4096;
constexpr unsigned L_OX = L_Q + 1024;
constexpr unsigned L_M = L_OX + 512;
constexpr unsigned L_L = L_M + 128;
constexpr unsigned L_W = L_L + 128;
constexpr unsigned L_H = L_W + 128;
constexpr unsigned L_XB = L_H + 64;
constexpr unsigned L_R = L_XB + 32;
constexpr unsigned L_FLAG = L_R + 64;
constexpr unsigned L_BYTES = L_FLAG + 64;
static_assert(L_BYTES <= 160 * 1024, "LDS");
enum { F_VP = 0, F_Q = 4, F_MAX = 5, F_P = 6, F_W = 7, F_RED = 12, F_KISS = 13, F_TAGA = 8, F_TAGB = 9, F_EP0 = 10, F_EP1 = 11 };

__device__ __forceinline__ float bf2f(unsigned b) { return __builtin_bit_cast(float, b << 16); }
__device__ __forceinline__ float bflo(unsigned u) { return __builtin_bit_cast(float, u << 16); }
__device__ __forceinline__ float bfhi(unsigned u) { return __builtin_bit_cast(float, u & 0xffff0000u); }
__device__ __forceinline__ unsigned f2bf(float f) { return (unsigned)__builtin_bit_cast(u16, (__bf16)f); }
__device__ __forceinline__ float rbf(float f) { return bf2f(f2bf(f)); }
__device__ __forceinline__ unsigned pack2(float lo, float hi) {
  typedef __bf16 bf16x2 __attribute__((ext_vector_type(2)));
  const f32x2 v = {lo, hi};
  return __builtin_bit_cast(unsigned, __builtin_convertvector(v, bf16x2));
}
__device__ __forceinline__ f32x4 mfma32(u32x4 a, u32x4 b, f32x4 c) {
  return __builtin_amdgcn_mfma_f32_16x16x32_bf16(__builtin_bit_cast(bf16x8, a), __builtin_bit_cast(bf16x8, b), c, 0, 0, 0);
}
__device__ __forceinline__ f32x4 mfma16(u32x2 a, u32x2 b, f32x4 c) {
  return __builtin_amdgcn_mfma_f32_16x16x16bf16_1k(__builtin_bit_cast(s16x4, a), __builtin_bit_cast(s16x4, b), c, 0, 0, 0);
}
template <int CTRL> __device__ __forceinline__ float dpp(float x) {
  return __builtin_bit_cast(float, __builtin_amdgcn_mov_dpp(__builtin_bit_cast(int, x), CTRL, 0xf, 0xf, true));
}
constexpr int XOR1 = 0xB1, XOR2 = 0x4E, HMIR = 0x141, MIR = 0x140, ROR4 = 0x124, ROR8 = 0x128;
__device__ __forceinline__ float row_sum(float v) { v += dpp<XOR1>(v); v += dpp<XOR2>(v); v += dpp<HMIR>(v); v += dpp<MIR>(v); return v; }
__device__ __forceinline__ float row_max(float v) {
  v = fmaxf(v, dpp<XOR1>(v)); v = fmaxf(v, dpp<XOR2>(v)); v = fmaxf(v, dpp<HMIR>(v)); v = fmaxf(v, dpp<MIR>(v)); return v;
}
__device__ __forceinline__ float x16_sum(float x) {
  auto s = __builtin_amdgcn_permlane16_swap(__float_as_uint(x), __float_as_uint(x), false, false);
  return __uint_as_float(s[0]) + __uint_as_float(s[1]);
}
__device__ __forceinline__ float x32_sum(float x) {
  auto t = __builtin_amdgcn_permlane32_swap(__float_as_uint(x), __float_as_uint(x), false, false);
  return __uint_as_float(t[0]) + __uint_as_float(t[1]);
}
__device__ __forceinline__ float xrow_sum(float x) { return x32_sum(x16_sum(x)); }
__device__ __forceinline__ float xrow_max(float x) {
  auto s = __builtin_amdgcn_permlane16_swap(__float_as_uint(x), __float_as_uint(x), false, false);
  x = fmaxf(__uint_as_float(s[0]), __uint_as_float(s[1]));
  auto t = __builtin_amdgcn_permlane32_swap(__float_as_uint(x), __float_as_uint(x), false, false);
  return fmaxf(__uint_as_float(t[0]), __uint_as_float(t[1]));
}
__device__ __forceinline__ float ex2(float x) { return __builtin_amdgcn_exp2f(x); }

#define STR_(x) #x
#define STR(x) STR_(x)
#define CBAR() asm volatile("" ::: "memory")
#define SCHED() __builtin_amdgcn_sched_barrier(0)
#define WAIT_VM(n) asm volatile("s_waitcnt vmcnt(" #n ")" ::: "memory")
#define BARRIER() do { asm volatile("s_waitcnt lgkmcnt(0)" ::: "memory"); __builtin_amdgcn_s_barrier(); CBAR(); } while (0)
#define LDS_SPIN(cond) do { _Pragma("nounroll") for (unsigned spins_ = 0; spins_ < (SPIN_CAP << 4) && (cond); ++spins_) __builtin_amdgcn_s_sleep(SPIN_SLEEP); CBAR(); } while (0)
#define LDS_SPIN_HOT(cond) do { _Pragma("nounroll") for (unsigned spins_ = 0; spins_ < (SPIN_CAP << 5) && (cond); ++spins_) {} CBAR(); } while (0)
#define VPROG() vprog(*(volatile LAS u32x4*)(L + L_FLAG))
#define LDS_INC(i) do { CBAR(); if (lane == 0) __hip_atomic_fetch_add((LAS unsigned*)(L + L_FLAG + 4 * (i)), 1u, __ATOMIC_RELAXED, __HIP_MEMORY_SCOPE_WORKGROUP); CBAR(); } while (0)

__device__ __forceinline__ void dma4_kv(const void* sbase, unsigned v0, unsigned v1, unsigned v2, unsigned v3, unsigned dst) {
  unsigned keep;
  asm volatile(
      "s_mov_b32 %0, m0\n\ts_mov_b32 m0, %6\n\ts_nop 0\n\t"
      "global_load_lds_dwordx4 %1, %5 nt\n\t"
      "global_load_lds_dwordx4 %2, %5 offset:1024 nt\n\t"
      "global_load_lds_dwordx4 %3, %5 offset:2048 nt\n\t"
      "global_load_lds_dwordx4 %4, %5 offset:3072 nt\n\t"
      "s_mov_b32 m0, %0"
      : "=&s"(keep) : "v"(v0), "v"(v1), "v"(v2), "v"(v3), "s"(sbase), "s"(dst) : "memory");
}
#define DMA_STEP "global_load_lds_dwordx4 %1, %2\n\ts_add_u32 m0, m0, 0x400\n\tv_add_u32 %1, %4, %1\n\t"
__device__ __forceinline__ void dma8_w(const void* sbase, unsigned& voff, unsigned step, unsigned dst) {
  unsigned keep;
  asm volatile("s_mov_b32 %0, m0\n\ts_mov_b32 m0, %3\n\ts_nop 0\n\t" DMA_STEP DMA_STEP DMA_STEP DMA_STEP DMA_STEP DMA_STEP DMA_STEP DMA_STEP "s_mov_b32 m0, %0"
               : "=&s"(keep), "+v"(voff) : "s"(sbase), "s"(dst), "s"(step) : "memory", "scc");
}
__device__ __forceinline__ void dma1_w(const void* sbase, unsigned voff, unsigned dst) {
  unsigned keep;
  asm volatile("s_mov_b32 %0, m0\n\ts_mov_b32 m0, %3\n\ts_nop 0\n\tglobal_load_lds_dwordx4 %1, %2\n\ts_mov_b32 m0, %0"
               : "=&s"(keep) : "v"(voff), "s"(sbase), "s"(dst) : "memory");
}
__device__ __forceinline__ u32x2 tr_read(LAS unsigned char* L, unsigned a) {
  return __builtin_bit_cast(u32x2, __builtin_amdgcn_ds_read_tr16_b64_v4i16((LAS s16x4*)(L + a)));
}
__device__ __forceinline__ unsigned g_lo(float v, unsigned tagb) {
  unsigned u = __builtin_bit_cast(unsigned, v);
  u += 0x7fu + ((u >> 8) & 1u);
  return (u & 0xffffff00u) | tagb;
}
__device__ __forceinline__ float g_val(unsigned lo) { return __builtin_bit_cast(float, lo & 0xffffff00u); }
__device__ __forceinline__ unsigned g_bad(u32x4 v, unsigned taga, unsigned tagb) {
  return (v[1] ^ taga) | (v[3] ^ taga) | (((v[0] ^ tagb) | (v[2] ^ tagb)) & 0xffu);
}
__device__ __forceinline__ unsigned vprog(u32x4 v) { return min(min(v[0], v[1]), min(v[2], v[3])); }
constexpr int AUX_SC1 = 16;

__device__ __forceinline__ u64 poll_pair(const void* sbase, unsigned voff, unsigned taga, unsigned tagb, float& r0, float& r1, u64 pend, unsigned left) {
  unsigned t0, t1, t2;
  u64 sv;
  unsigned cnt, nb;
#define PCHK(R0, R1, R2, R3, RR)                                       \
  "s_waitcnt vmcnt(" STR(PDEPTH) "-1)\n\t"                             \
  "v_xor_b32 %[t0], " R1 ", %[ta]\n\t"                                 \
  "v_xor_b32 %[t1], " R0 ", %[tb]\n\t"                                 \
  "v_xor_b32 %[t2], " R2 ", %[tb]\n\t"                                 \
  "v_or_b32 %[t1], %[t1], %[t2]\n\t"                                   \
  "v_xor_b32 %[t2], " R3 ", %[ta]\n\t"                                 \
  "v_and_b32 %[t1], 0xff, %[t1]\n\t"                                   \
  "v_or3_b32 %[t0], %[t0], %[t1], %[t2]\n\t"                           \
  "v_cmp_eq_u32 vcc, 0, %[t0]\n\t"                                     \
  "s_mov_b64 exec, vcc\n\t"                                            \
  "v_and_b32 %[r0], 0xffffff00, " R0 "\n\t"                            \
  "v_and_b32 %[r1], 0xffffff00, " R2 "\n\t"                            \
  "s_andn2_b64 %[pe], %[pe], vcc\n\t"                                  \
  "s_bcnt1_i32_b64 %[nb], %[pe]\n\t"                                   \
  "s_cmp_le_u32 %[nb], %[lf]\n\t"                                      \
  "s_cbranch_scc1 2f\n\t"                                              \
  "s_mov_b64 exec, %[pe]\n\t"                                          \
  "global_load_dwordx4 " RR ", %[vo], %[sb] sc1\n\t"
  asm volatile(
      "s_mov_b64 %[sv], exec\n\t"
      "s_mov_b64 exec, %[pe]\n\t"
      "s_mov_b32 %[cn], 0x10000\n\t"
      "global_load_dwordx4 v[100:103], %[vo], %[sb] sc1\n\t"
      "1:\n\t"
      PCHK("v100", "v101", "v102", "v103", "v[100:103]")
      "s_sub_u32 %[cn], %[cn], 1\n\t"
      "s_cbranch_scc0 1b\n\t"
      "2:\n\t"
      "s_mov_b64 exec, %[sv]\n\t"
      : [r0] "+v"(r0), [r1] "+v"(r1), [t0] "=&v"(t0), [t1] "=&v"(t1), [t2] "=&v"(t2), [sv] "=&s"(sv), [pe] "+s"(pend), [cn] "=&s"(cnt), [nb] "=&s"(nb)
      : [vo] "v"(voff), [sb] "s"(sbase), [ta] "v"(taga), [tb] "v"(tagb), [lf] "s"(left)
      : "memory", "vcc", "scc", "v100", "v101", "v102", "v103", "v104", "v105", "v106", "v107", "v108", "v109", "v110", "v111");
  return pend;
#undef PCHK
}

#define STAMP(i) do {} while (0)
#define STAMP_DECL() do {} while (0)
#define STAMP_FLUSH() do {} while (0)

__device__ __forceinline__ float gelu_tanh(float x) {
  const float u = 0.7978845608028654f * (x + 0.044715f * x * x * x);
  const float e = ex2(u * (2.0f * LOG2E));
  const float t = 1.0f - 2.0f * __builtin_amdgcn_rcpf(e + 1.0f);
  return 0.5f * x * (1.0f + t);
}

__global__ void __launch_bounds__(NTHR) __attribute__((amdgpu_num_vgpr(100)))
fused_kernel(const u16* __restrict__ x, const u16* __restrict__ kc, const u16* __restrict__ vc, const u16* __restrict__ wq,
             const u16* __restrict__ wo, const u16* __restrict__ w1, const u16* __restrict__ b1, const u16* __restrict__ w2,
             const u16* __restrict__ b2, u16* __restrict__ out, unsigned char* __restrict__ ws) {
  extern __shared__ __attribute__((aligned(16))) unsigned char smem[];
  LAS unsigned char* L = (LAS unsigned char*)smem;
  const unsigned lbase = (unsigned)(size_t)L;
  volatile LAS unsigned* flags = (volatile LAS unsigned*)(L + L_FLAG);
  const int tid = threadIdx.x, lane = tid & 63, wv = __builtin_amdgcn_readfirstlane(tid >> 6);
  const int g = blockIdx.x & 7, j = blockIdx.x >> 3;
  const int b = j >> 4, sp = j & 15, rb = j >> 1, ch = j & 1;
  const int lg = lane >> 4, lr = lane & 15;
  const int me = g * 32 + j;
  const int rch = me >> 7, ru = (me & 127) * 2 + (lane & 1);
  const int rcol = rch * 256 + (ru >> 5) * 32 + (ru & 15), rbt = (ru >> 4) & 1;
  const size_t kvoff = ((size_t)(b * HKV + g) * SK + sp * KEYS) * (DH * 2);
  const __amdgpu_buffer_rsrc_t rs = __builtin_amdgcn_make_buffer_rsrc((void*)ws, 0, (int)WS_END, 0x00020000);
  asm volatile("" ::"s"(x), "s"(kc), "s"(vc), "s"(wq), "s"(wo), "s"(w1), "s"(b1), "s"(w2), "s"(b2), "s"(out), "s"(ws));
  STAMP_DECL();
  STAMP(0);

  if (wv < 2) {
    __builtin_amdgcn_s_setprio(3);
    const bool isq = (wv == 0);
    const bool hasw1 = (j >> 2) != 3;
    const int p1 = hasw1 ? (j < 12 ? j : j - 4) * 8 + g : 0;
    const int rb2 = rb < 14 ? rb : 0;
    u32x4 xr0, xr1;
    u32x2 ep;
    unsigned bv;
    {
      const unsigned xo = lane * 32, zo = 0u, bo = (unsigned)((g * 224 + rb2 * 16 + 2 * (lane & 7)) * 2);
      asm volatile("global_load_dwordx4 %0, %4, %7\n\tglobal_load_dwordx4 %1, %4, %7 offset:16\n\t"
                   "global_load_dwordx2 %2, %5, %8 sc1\n\tglobal_load_dword %3, %6, %9"
                   : "=&v"(xr0), "=&v"(xr1), "=&v"(ep), "=&v"(bv) : "v"(xo), "v"(zo), "v"(bo), "s"(x), "s"(ws), "s"(b1) : "memory");
    }
    const unsigned ld = isq ? HQ * DH * 2 : FF * 2;
    const int k0 = isq ? (j & 1) * 256 : (p1 & 3) * 128;
    const unsigned char* wsrc = (isq ? (const unsigned char*)(wq + g * 512 + (j >> 1) * 32) : (const unsigned char*)(w1 + (p1 >> 2) * 32)) + (size_t)k0 * ld;
    const unsigned stg = isq ? L_WST : L_WST + 16384;
    {
      unsigned vo = (lane >> 2) * ld + (lane & 3) * 16;
      if (isq && lane < 16) flags[lane] = 0u;
      if (isq || hasw1) dma8_w(wsrc, vo, 16 * ld, lbase + stg);
      if (isq) dma8_w(wsrc, vo, 16 * ld, lbase + stg + 8192);
      BARRIER();
      if (!isq && !hasw1) WAIT_VM(0);
    }
    STAMP(1);
    if (isq) WAIT_VM(16); else WAIT_VM(8);
    asm volatile("; PIN x" : "+v"(xr0), "+v"(xr1), "+v"(ep), "+v"(bv)::"memory");
    {
      float f[16];
#pragma unroll
      for (int i = 0; i < 4; ++i) { f[2 * i] = bflo(xr0[i]); f[2 * i + 1] = bfhi(xr0[i]); f[8 + 2 * i] = bflo(xr1[i]); f[9 + 2 * i] = bfhi(xr1[i]); }
      float s = 0.f;
#pragma unroll
      for (int i = 0; i < 16; ++i) s += f[i];
      const float mean = x16_sum(row_sum(s)) * (1.0f / DM);
      float q = 0.f;
#pragma unroll
      for (int i = 0; i < 16; ++i) { const float d = f[i] - mean; q += d * d; }
      const float var = x16_sum(row_sum(q)) * (1.0f / DM);
      const float mb = rbf(mean);
      const float inv = __builtin_amdgcn_rcpf(rbf(__builtin_amdgcn_sqrtf(rbf(rbf(var) + 1e-5f))));
      u32x4 o0, o1;
#pragma unroll
      for (int i = 0; i < 4; ++i) {
        const unsigned d0 = pack2(f[2 * i] - mb, f[2 * i + 1] - mb), d1 = pack2(f[8 + 2 * i] - mb, f[9 + 2 * i] - mb);
        o0[i] = pack2(bflo(d0) * inv, bfhi(d0) * inv);
        o1[i] = pack2(bflo(d1) * inv, bfhi(d1) * inv);
      }
      *(LAS u32x4*)(L + L_XN + wv * 2048 + lane * 32) = o0;
      *(LAS u32x4*)(L + L_XN + wv * 2048 + lane * 32 + 16) = o1;
    }
    const unsigned taga = ep[0] + 1u, tagb = (ep[1] + (taga == 0u ? 1u : 0u)) & 0xffu;
    STAMP(2);
    f32x4 acc[2] = {f32x4{0.f, 0.f, 0.f, 0.f}, f32x4{0.f, 0.f, 0.f, 0.f}};
    {
      unsigned ta = stg + (8 * lg + (lr >> 2)) * 64 + 8 * (lr & 3);
      unsigned xa = L_XN + wv * 2048 + (lr & 1) * 1024 + k0 * 2 + lg * 16;
      if (isq) WAIT_VM(8); else WAIT_VM(0);
      _Pragma("nounroll") for (int hb = 0; hb < 2; ++hb) {
#pragma unroll
        for (int s = 0; s < 4; ++s) {
          const u32x4 xf = *(LAS u32x4*)(L + xa + s * 64);
#pragma unroll
          for (int t = 0; t < 2; ++t) {
            const u32x2 lo = tr_read(L, ta + s * 2048 + t * 32), hi = tr_read(L, ta + s * 2048 + t * 32 + 256);
            acc[t] = mfma32(u32x4{lo[0], lo[1], hi[0], hi[1]}, xf, acc[t]);
          }
        }
        if (!isq) break;
        ta += 8192; xa += 256;
        WAIT_VM(0);
      }
    }
    STAMP(3);
    if (lr < 8 && (isq || hasw1)) {
      const f32x4 a = (lr & 4) ? acc[1] : acc[0];
      const float v0 = (lr & 2) ? a[2] : a[0], v1 = (lr & 2) ? a[3] : a[1];
      const u32x4 gv = {g_lo(v0, tagb), taga, g_lo(v1, tagb), taga};
      const unsigned pb = isq ? WS_Q + (g * 2 + (j & 1)) * 4096 + (j >> 1) * 256 : WS_HP + (p1 >> 2) * 2048 + (p1 & 3) * 256;
      const unsigned sb = isq ? 65536u : 128u, st = isq ? 128u : 1024u;
      __builtin_amdgcn_raw_buffer_store_b128(gv, rs, (int)(pb + (lr & 1) * sb + (lr >> 2) * st + lg * 32 + (lr & 2) * 8), 0, AUX_SC1);
    }
    STAMP(4);
    __builtin_amdgcn_s_sleep(QSLEEP);
    {
      const int qo = (int)(WS_Q + (b * 8 + g) * 8192) + wv * 2048 + lane * 16;
      u32x4 qv[4];
      bool ok[4];
#pragma unroll
      for (int i = 0; i < 4; ++i) { qv[i] = u32x4{0u, 0u, 0u, 0u}; ok[i] = false; }
      _Pragma("nounroll") for (unsigned spins = 0; spins < AUX_CAP; ++spins) {
        CBAR();
#pragma unroll
        for (int i = 0; i < 4; ++i) if (!ok[i]) qv[i] = __builtin_amdgcn_raw_buffer_load_b128(rs, qo + (i & 1) * 1024 + (i >> 1) * 4096, 0, AUX_SC1);
        bool all = true;
#pragma unroll
        for (int i = 0; i < 4; ++i) { ok[i] = g_bad(qv[i], taga, tagb) == 0u; all = all && ok[i]; }
        if (__all(all)) break;
      }
#pragma unroll
      for (int i = 0; i < 2; ++i)
        *(LAS unsigned*)(L + L_Q + wv * 512 + i * 256 + lane * 4) = pack2(g_val(qv[i][0]) + g_val(qv[2 + i][0]), g_val(qv[i][2]) + g_val(qv[2 + i][2]));
      if (isq && lane == 0) { flags[F_TAGA] = taga; flags[F_TAGB] = tagb; flags[F_EP0] = ep[0]; flags[F_EP1] = ep[1]; }
      LDS_INC(F_Q);
    }
    STAMP(5);
    if (!isq) {
      if (lane < 2) {
        const float xb0 = bf2f(x[rbt * DM + rcol]) + bf2f(b2[rcol]), xb1 = bf2f(x[rbt * DM + rcol + 16]) + bf2f(b2[rcol + 16]);
        *(LAS f32x2*)(L + L_XB + lane * 8) = f32x2{xb0, xb1};
      }
      unsigned hv = 0u;
      if (rb < 14) {
        const int ho = (int)(WS_HP + (g * 14 + rb) * 1024) + lane * 16;
        u32x4 h0 = {0u, 0u, 0u, 0u};
        bool ok0 = false;
        _Pragma("nounroll") for (unsigned spins = 0; spins < AUX_CAP; ++spins) {
          CBAR();
          if (!ok0) h0 = __builtin_amdgcn_raw_buffer_load_b128(rs, ho, 0, AUX_SC1);
          ok0 = g_bad(h0, taga, tagb) == 0u;
          if (__all(ok0)) break;
        }
        const float a0 = xrow_sum(g_val(h0[0])), a1 = xrow_sum(g_val(h0[2]));
        hv = pack2(gelu_tanh(rbf(rbf(a0) + bflo(bv))), gelu_tanh(rbf(rbf(a1) + bfhi(bv))));
      }
      if (lane < 16) *(LAS unsigned*)(L + L_H + lane * 4) = hv;
      STAMP(6);
      STAMP_FLUSH();
      return;
    }
    LDS_SPIN(flags[F_P] < 8u);
    STAMP(6);
    {
      const f32x4 m0 = *(LAS f32x4*)(L + L_M + (lane & 3) * 32), m1 = *(LAS f32x4*)(L + L_M + (lane & 3) * 32 + 16);
      const f32x4 l0 = *(LAS f32x4*)(L + L_L + (lane & 3) * 32), l1 = *(LAS f32x4*)(L + L_L + (lane & 3) * 32 + 16);
      const float mc = fmaxf(fmaxf(fmaxf(m0[0], m0[1]), fmaxf(m0[2], m0[3])), fmaxf(fmaxf(m1[0], m1[1]), fmaxf(m1[2], m1[3])));
      const float lc = ((l0[0] + l0[1]) + (l0[2] + l0[3])) + ((l1[0] + l1[1]) + (l1[2] + l1[3]));
      if (lane < 4) {
        const u32x4 gv = {g_lo(mc, tagb), taga, g_lo(lc, tagb), taga};
        __builtin_amdgcn_raw_buffer_store_b128(gv, rs, (int)(WS_ML + ((g * 4 + lane) * 32 + j) * 16), 0, AUX_SC1);
      }
      STAMP(7);
      __builtin_amdgcn_s_sleep(QSLEEP);
      const int mo = (int)(WS_ML + ((g * 4 + (rb >> 2)) * 32 + (lane & 31)) * 16);
      u32x4 gm = {0u, 0u, 0u, 0u};
      bool ok = false;
      _Pragma("nounroll") for (unsigned spins = 0; spins < AUX_CAP; ++spins) {
        CBAR();
        if (!ok) gm = __builtin_amdgcn_raw_buffer_load_b128(rs, mo, 0, AUX_SC1);
        ok = g_bad(gm, taga, tagb) == 0u;
        if (__all(ok)) break;
        __builtin_amdgcn_s_sleep(8);
      }
      const float ms = g_val(gm[0]);
      const float e = ex2((ms - row_max(ms)) * LOG2E);
      const float w = e / row_sum(e * g_val(gm[2]));
      if (lane < 32) *(LAS float*)(L + L_W + lane * 4) = w;
      CBAR();
      if (lane == 0) flags[F_W] = 1u;
    }
    STAMP(8);
    STAMP_FLUSH();
    return;
  }

  if (wv < NAUX) {
    const int ld = wv - 2;
    volatile LAS unsigned* prog = flags + F_VP + ld;
    {
      const unsigned char* vbase = (const unsigned char*)vc + kvoff + ld * 4096;
      const unsigned dst = lbase + L_V + ld * 4096;
      const unsigned ve = lg * 256 + ((((lr >> 1) ^ lg) << 1) | (lr & 1)) * 16, vo = ve ^ 128u;
      _Pragma("nounroll") for (int c = 0; c < 8; ++c) {
        if (c == INITV) { BARRIER(); LDS_SPIN(flags[F_KISS] < (unsigned)KTRIG); STAMP(1); }
        dma4_kv(vbase + c * 16384, ve, vo, ve, vo, dst + c * 16384);
      }
    }
    STAMP(3);
    {
      LDS_SPIN(flags[F_Q] < 2u);
      const int hl = lane >> 5, slot = lane & 31, rb2 = rb < 14 ? rb : 0;
      const unsigned char* wob = (const unsigned char*)(wo + (size_t)(g * 512 + rb * 32 + ld * 8) * DM + ch * 256);
      const unsigned char* w2b = (const unsigned char*)(w2 + (size_t)(g * 224 + rb2 * 16 + ld * 4) * DM + ch * 256);
      const unsigned base = hl * 1024 + slot * 16;
      _Pragma("nounroll") for (int i = 0; i < 4; ++i)
        dma1_w(wob + i * 2048, base ^ ((((ld & 1) << 2) | ((i & 1) << 1) | hl) << 5), lbase + L_WST + ld * 4096 + i * 1024);
      _Pragma("nounroll") for (int i = 0; i < 2; ++i)
        dma1_w(w2b + i * 2048, base ^ (((4 * ld + 2 * i + hl) & 7) << 5), lbase + L_WST + 16384 + ld * 2048 + i * 1024);
    }
    STAMP(4);
    WAIT_VM(22); *prog = 4u;
    WAIT_VM(14); *prog = 6u;
    WAIT_VM(10); *prog = 7u;
    WAIT_VM(6); *prog = 8u;
    STAMP(5);
    WAIT_VM(0);
    STAMP(6);
    STAMP_FLUSH();
    return;
  }

  const int sw = wv - NAUX;
  __builtin_amdgcn_s_setprio(2);
  BARRIER();
  u32x4 kf[4][4];
  {
    const unsigned char* kbase = (const unsigned char*)kc + kvoff + sw * 4096;
    const unsigned ko = lr * 256 + lg * 16;
#pragma unroll
    for (int t = 0; t < 4; ++t)
      asm volatile("global_load_dwordx4 %0, %4, %5 nt\n\tglobal_load_dwordx4 %1, %4, %5 offset:64 nt\n\t"
                   "global_load_dwordx4 %2, %4, %5 offset:128 nt\n\tglobal_load_dwordx4 %3, %4, %5 offset:192 nt"
                   : "=&v"(kf[t][0]), "=&v"(kf[t][1]), "=&v"(kf[t][2]), "=&v"(kf[t][3]) : "v"(ko), "s"(kbase + t * 32768) : "memory");
  }
  LDS_INC(F_KISS);
  STAMP(1);
  LDS_SPIN(flags[F_Q] < 2u);
  u32x4 qf[4];
#pragma unroll
  for (int s = 0; s < 4; ++s) qf[s] = *(LAS u32x4*)(L + L_Q + (lr & 3) * 256 + s * 64 + lg * 16);
  const unsigned taga = flags[F_TAGA], tagb = flags[F_TAGB];
  asm volatile("s_waitcnt vmcnt(0)"
               : "+v"(kf[0][0]), "+v"(kf[0][1]), "+v"(kf[0][2]), "+v"(kf[0][3]), "+v"(kf[1][0]), "+v"(kf[1][1]), "+v"(kf[1][2]), "+v"(kf[1][3]),
                 "+v"(kf[2][0]), "+v"(kf[2][1]), "+v"(kf[2][2]), "+v"(kf[2][3]), "+v"(kf[3][0]), "+v"(kf[3][1]), "+v"(kf[3][2]), "+v"(kf[3][3])
               :: "memory");
  STAMP(2);
  {
    float sc[4][4];
    float m = -INFINITY;
#pragma unroll
    for (int t = 0; t < 4; ++t) {
      f32x4 a = {0.f, 0.f, 0.f, 0.f};
#pragma unroll
      for (int s = 0; s < 4; ++s) a = mfma32(kf[t][s], qf[s], a);
      const unsigned r01 = pack2(a[0], a[1]), r23 = pack2(a[2], a[3]);
      const unsigned s01 = pack2(bflo(r01) * SCALE, bfhi(r01) * SCALE), s23 = pack2(bflo(r23) * SCALE, bfhi(r23) * SCALE);
      sc[t][0] = bflo(s01); sc[t][1] = bfhi(s01); sc[t][2] = bflo(s23); sc[t][3] = bfhi(s23);
      m = fmaxf(fmaxf(m, sc[t][0]), fmaxf(sc[t][1], fmaxf(sc[t][2], sc[t][3])));
    }
    m = xrow_max(m);
    if (lane < 4) *(LAS float*)(L + L_M + lane * 32 + sw * 4) = m;
    LDS_INC(F_MAX);
    LDS_SPIN(flags[F_MAX] < 8u);
    {
      const f32x4 m0 = *(LAS f32x4*)(L + L_M + (lr & 3) * 32), m1 = *(LAS f32x4*)(L + L_M + (lr & 3) * 32 + 16);
      m = fmaxf(fmaxf(fmaxf(m0[0], m0[1]), fmaxf(m0[2], m0[3])), fmaxf(fmaxf(m1[0], m1[1]), fmaxf(m1[2], m1[3])));
    }
    float l = 0.f;
#pragma unroll
    for (int t = 0; t < 4; ++t) {
#pragma unroll
      for (int r = 0; r < 4; ++r) { sc[t][r] = ex2((sc[t][r] - m) * LOG2E); l += sc[t][r]; }
      if (lr < 4) *(LAS u32x2*)(L + L_P + (4 * t + (sw >> 1)) * 256 + lr * 64 + lg * 16 + (sw & 1) * 8) = u32x2{pack2(sc[t][0], sc[t][1]), pack2(sc[t][2], sc[t][3])};
    }
    l = xrow_sum(l);
    if (lane < 4) *(LAS float*)(L + L_L + lane * 32 + sw * 4) = l;
    LDS_INC(F_P);
  }
  STAMP(3);
  LDS_SPIN(flags[F_P] < 8u);
  f32x4 o = {0.f, 0.f, 0.f, 0.f};
  {
    u32x4 pf[16];
#pragma unroll
    for (int s = 0; s < 16; ++s) pf[s] = *(LAS u32x4*)(L + L_P + s * 256 + (lr & 3) * 64 + lg * 16);
    const unsigned tb = L_V + (4 * lg + (lr >> 2)) * 256 + ((sw ^ (4 * (lg & 1) + (lr >> 2))) << 5) + 8 * (lr & 3);
#pragma unroll
    for (int s = 0; s < 16; ++s) {
      if (s == 0) { LDS_SPIN(VPROG() < 4u); STAMP(4); }
      if (s == 8) LDS_SPIN(VPROG() < 6u);
      if (s == 12) LDS_SPIN_HOT(VPROG() < 7u);
      if (s == 14) { LDS_SPIN_HOT(VPROG() < 8u); STAMP(5); }
      const u32x2 lo = tr_read(L, tb + s * 8192), hi = tr_read(L, tb + s * 8192 + 4096);
      o = mfma32(u32x4{lo[0], lo[1], hi[0], hi[1]}, pf[s], o);
      if (s == 7 || s == 11 || s == 13) SCHED();
    }
  }
  if (lr < 8) {
    const float v0 = (lr & 4) ? o[2] : o[0], v1 = (lr & 4) ? o[3] : o[1];
    const u32x4 gv = {g_lo(v0, tagb), taga, g_lo(v1, tagb), taga};
    __builtin_amdgcn_raw_buffer_store_b128(gv, rs, (int)(WS_H1 + me * 4096 + sw * 128) + (lr & 3) * 1024 + lg * 32 + (lr & 4) * 4, 0, AUX_SC1);
  }
  STAMP(7);
  {
    LDS_SPIN_HOT(flags[F_W] == 0u);
    const float wt = *(LAS float*)(L + L_W + (sw * 4 + lg) * 4);
    const unsigned oo = WS_H1 + ((g * 32 + sw * 4 + lg) * 4 + (rb >> 2)) * 1024 + (rb & 3) * 256 + lr * 16;
    float v0 = 0.f, v1 = 0.f;
    poll_pair(ws, oo, taga, tagb, v0, v1, ~0ull, 0u);
    STAMP(8);
    v0 = xrow_sum(wt * v0); v1 = xrow_sum(wt * v1);
    if (lane < 16) *(LAS unsigned*)(L + L_OX + sw * 64 + lr * 4) = pack2(v0, v1);
  }
  BARRIER();
  {
    const u32x4 of = *(LAS u32x4*)(L + L_OX + (lr & 7) * 64 + lg * 16);
    const int q = lr >> 2;
    const unsigned ta = L_WST + (8 * lg + q) * 512 + (((2 * sw) ^ (q | ((lg & 1) << 2))) << 5) + 8 * (lr & 3);
    const unsigned t2 = L_WST + 16384 + (4 * lg + q) * 512 + (((2 * sw) ^ ((4 * lg + q) & 7)) << 5) + 8 * (lr & 3);
    f32x4 acc[2], mac[2];
    const u32x2 hf = *(LAS u32x2*)(L + L_H + (q & 1) * 32 + lg * 8);
#pragma unroll
    for (int t = 0; t < 2; ++t) {
      const u32x2 lo = tr_read(L, ta ^ (t << 5)), hi = tr_read(L, (ta ^ (t << 5)) + 2048);
      const u32x2 w2f = tr_read(L, t2 ^ (t << 5));
      acc[t] = mfma32(of, u32x4{lo[0], lo[1], hi[0], hi[1]}, f32x4{0.f, 0.f, 0.f, 0.f});
      mac[t] = mfma16(hf, w2f, f32x4{0.f, 0.f, 0.f, 0.f});
    }
    if (lane < 32) {
      const float r0 = (acc[0][0] + acc[0][1]) + (acc[0][2] + acc[0][3]) + mac[0][0], r1 = (acc[1][0] + acc[1][1]) + (acc[1][2] + acc[1][3]) + mac[1][0];
      const u32x4 gv = {g_lo(r0, tagb), taga, g_lo(r1, tagb), taga};
      __builtin_amdgcn_raw_buffer_store_b128(gv, rs, (int)(WS_H2 + ((ch * 128 + g * 16 + rb) * 8 + sw) * 512) + lane * 16, 0, AUX_SC1);
    }
  }
  STAMP(9);
  if (sw < 4) {
    const unsigned ho = WS_H2 + (rch * 128 + (lane >> 3) * 16 + sw * 4 + ((lane >> 1) & 3)) * 4096 + ru * 16;
    float s0 = 0.f, s1 = 0.f;
    const u64 miss = poll_pair(ws, ho, taga, tagb, s0, s1, 0x0101010101010101ull, 1u);
    poll_pair(ws, ho, taga, tagb, s0, s1, miss | ~0x0101010101010101ull, 0u);
    STAMP(10);
    s0 += dpp<XOR2>(s0); s1 += dpp<XOR2>(s1);
    s0 += dpp<ROR4>(s0); s1 += dpp<ROR4>(s1);
    s0 += dpp<ROR8>(s0); s1 += dpp<ROR8>(s1);
    s0 = xrow_sum(s0); s1 = xrow_sum(s1);
    if (lane < 2) *(LAS f32x2*)(L + L_R + sw * 16 + lane * 8) = f32x2{s0, s1};
    CBAR();
    unsigned old = 0u;
    if (lane == 0) old = __hip_atomic_fetch_add((LAS unsigned*)(L + L_FLAG + 4 * F_RED), 1u, __ATOMIC_RELAXED, __HIP_MEMORY_SCOPE_WORKGROUP);
    CBAR();
    if (__builtin_amdgcn_readfirstlane(old) == 3u) {
      if (lane < 2) {
        f32x2 r = *(LAS f32x2*)(L + L_XB + lane * 8);
#pragma unroll
        for (int k = 0; k < 4; ++k) r += *(LAS f32x2*)(L + L_R + k * 16 + lane * 8);
        out[rbt * DM + rcol] = (u16)f2bf(r[0]);
        out[rbt * DM + rcol + 16] = (u16)f2bf(r[1]);
      }
      if (me == 0 && lane == 0) {
        const u64 e = (((u64)flags[F_EP1] << 32) | flags[F_EP0]) + 1ull;
        __hip_atomic_store((GAS u64*)(ws + WS_EPOCH), e, __ATOMIC_RELAXED, __HIP_MEMORY_SCOPE_AGENT);
      }
      STAMP(11);
    }
  }
  STAMP_FLUSH();
}

extern "C" void kernel_launch(void* const* d_in, const int* in_sizes, int n_in, void* d_out, int out_size, void* d_ws, size_t ws_size,
                              hipStream_t stream) {
  (void)hipFuncSetAttribute((const void*)fused_kernel, hipFuncAttributeMaxDynamicSharedMemorySize, L_BYTES);
  fused_kernel<<<256, NTHR, L_BYTES, stream>>>((const u16*)d_in[0], (const u16*)d_in[1], (const u16*)d_in[2], (const u16*)d_in[3],
                                               (const u16*)d_in[4], (const u16*)d_in[6], (const u16*)d_in[7], (const u16*)d_in[8],
                                               (const u16*)d_in[9], (u16*)d_out, (unsigned char*)d_ws);
}
```

```cpp
#include <hip/hip_runtime.h>
#include <hip/hip_bf16.h>
#include <stdint.h>

#define GAS __attribute__((address_space(1)))
#define LAS __attribute__((address_space(3)))
typedef unsigned u32x4 __attribute__((ext_vector_type(4)));
typedef unsigned u32x2 __attribute__((ext_vector_type(2)));
typedef float f32x4 __attribute__((ext_vector_type(4)));
typedef float f32x2 __attribute__((ext_vector_type(2)));
typedef short s16x4 __attribute__((ext_vector_type(4)));
typedef __bf16 bf16x8 __attribute__((ext_vector_type(8)));
typedef unsigned short u16;
typedef unsigned long long u64;

#define WPOS 8
#define H2LEFT 1
#define MLDEPTH 1
#define MLSLEEP 2
#define INITK 1
#define QSLEEP 64
#define MLTRIG 8
#define H1LEFT 3
#define PDEPTH 1
#define PSLEEP 4

constexpr int DM = 512, HQ = 32, HKV = 8, SK = 8192, DH = 128, FF = 1792;
constexpr int KEYS = 512;
constexpr int NAUX = 2, NSTR = 8, NLD = 4, NTHR = (NAUX + NSTR + NLD) * 64;
constexpr float SCALE = 0.08838834764831845f, LOG2E = 1.4426950408889634f;
constexpr unsigned SPIN_CAP = 1u << 17;
constexpr unsigned AUX_CAP = SPIN_CAP;
#define SPIN_SLEEP 1

constexpr unsigned WS_EPOCH = 0;
constexpr unsigned WS_ML = 393216;
constexpr unsigned WS_Q = 32768;
constexpr unsigned WS_HP = WS_Q + 131072;
constexpr unsigned WS_H1 = 524288;
constexpr unsigned WS_H2 = WS_H1 + (1u << 20);
constexpr unsigned WS_END = WS_H2 + (1u << 20);
constexpr unsigned WS_STAMP = 8u << 20;

constexpr unsigned L_V = 0;
constexpr unsigned L_WST = 131072;
constexpr unsigned L_XN = L_WST + 24576;
constexpr unsigned L_P = L_XN;
constexpr unsigned L_Q = L_XN + 4096;
constexpr unsigned L_OX = L_Q + 1024;
constexpr unsigned L_M = L_OX + 2048;
constexpr unsigned L_L = L_M + 128;
constexpr unsigned L_W = L_L + 128;
constexpr unsigned L_H = L_W + 128;
constexpr unsigned L_XB = L_H + 64;
constexpr unsigned L_R = L_XB + 32;
constexpr unsigned L_FLAG = L_R + 64;
constexpr unsigned L_BYTES = L_FLAG + 64;
static_assert(L_BYTES <= 160 * 1024, "LDS");
enum { F_VP = 0, F_Q = 4, F_MAX = 5, F_P = 6, F_W = 7, F_RED = 12, F_GO1 = 14, F_GO2 = 15, F_WIN = 13, F_TAGA = 8, F_TAGB = 9, F_EP0 = 10, F_EP1 = 11 };

__device__ __forceinline__ float bf2f(unsigned b) { return __builtin_bit_cast(float, b << 16); }
__device__ __forceinline__ float bflo(unsigned u) { return __builtin_bit_cast(float, u << 16); }
__device__ __forceinline__ float bfhi(unsigned u) { return __builtin_bit_cast(float, u & 0xffff0000u); }
__device__ __forceinline__ unsigned f2bf(float f) { return (unsigned)__builtin_bit_cast(u16, (__bf16)f); }
__device__ __forceinline__ float rbf(float f) { return bf2f(f2bf(f)); }
__device__ __forceinline__ unsigned pack2(float lo, float hi) {
  typedef __bf16 bf16x2 __attribute__((ext_vector_type(2)));
  const f32x2 v = {lo, hi};
  return __builtin_bit_cast(unsigned, __builtin_convertvector(v, bf16x2));
}
__device__ __forceinline__ f32x4 mfma32(u32x4 a, u32x4 b, f32x4 c) {
  return __builtin_amdgcn_mfma_f32_16x16x32_bf16(__builtin_bit_cast(bf16x8, a), __builtin_bit_cast(bf16x8, b), c, 0, 0, 0);
}
__device__ __forceinline__ f32x4 mfma16(u32x2 a, u32x2 b, f32x4 c) {
  return __builtin_amdgcn_mfma_f32_16x16x16bf16_1k(__builtin_bit_cast(s16x4, a), __builtin_bit_cast(s16x4, b), c, 0, 0, 0);
}
template <int CTRL> __device__ __forceinline__ float dpp(float x) {
  return __builtin_bit_cast(float, __builtin_amdgcn_mov_dpp(__builtin_bit_cast(int, x), CTRL, 0xf, 0xf, true));
}
constexpr int XOR1 = 0xB1, XOR2 = 0x4E, HMIR = 0x141, MIR = 0x140, ROR4 = 0x124, ROR8 = 0x128;
__device__ __forceinline__ float row_sum(float v) { v += dpp<XOR1>(v); v += dpp<XOR2>(v); v += dpp<HMIR>(v); v += dpp<MIR>(v); return v; }
__device__ __forceinline__ float row_max(float v) {
  v = fmaxf(v, dpp<XOR1>(v)); v = fmaxf(v, dpp<XOR2>(v)); v = fmaxf(v, dpp<HMIR>(v)); v = fmaxf(v, dpp<MIR>(v)); return v;
}
__device__ __forceinline__ float x16_sum(float x) {
  auto s = __builtin_amdgcn_permlane16_swap(__float_as_uint(x), __float_as_uint(x), false, false);
  return __uint_as_float(s[0]) + __uint_as_float(s[1]);
}
__device__ __forceinline__ float x32_sum(float x) {
  auto t = __builtin_amdgcn_permlane32_swap(__float_as_uint(x), __float_as_uint(x), false, false);
  return __uint_as_float(t[0]) + __uint_as_float(t[1]);
}
__device__ __forceinline__ float xrow_sum(float x) { return x32_sum(x16_sum(x)); }
__device__ __forceinline__ float xrow_max(float x) {
  auto s = __builtin_amdgcn_permlane16_swap(__float_as_uint(x), __float_as_uint(x), false, false);
  x = fmaxf(__uint_as_float(s[0]), __uint_as_float(s[1]));
  auto t = __builtin_amdgcn_permlane32_swap(__float_as_uint(x), __float_as_uint(x), false, false);
  return fmaxf(__uint_as_float(t[0]), __uint_as_float(t[1]));
}
__device__ __forceinline__ float ex2(float x) { return __builtin_amdgcn_exp2f(x); }

#define STR_(x) #x
#define STR(x) STR_(x)
#define CBAR() asm volatile("" ::: "memory")
#define SCHED() __builtin_amdgcn_sched_barrier(0)
#define WAIT_VM(n) asm volatile("s_waitcnt vmcnt(" #n ")" ::: "memory")
#define BARRIER() do { asm volatile("s_waitcnt lgkmcnt(0)" ::: "memory"); __builtin_amdgcn_s_barrier(); CBAR(); } while (0)
#define LDS_SPIN(cond) do { _Pragma("nounroll") for (unsigned spins_ = 0; spins_ < (SPIN_CAP << 4) && (cond); ++spins_) __builtin_amdgcn_s_sleep(SPIN_SLEEP); CBAR(); } while (0)
#define LDS_SPIN_HOT(cond) do { _Pragma("nounroll") for (unsigned spins_ = 0; spins_ < (SPIN_CAP << 5) && (cond); ++spins_) {} CBAR(); } while (0)
#define VPROG() vprog(*(volatile LAS u32x4*)(L + L_FLAG))
#define LDS_INC(i) do { CBAR(); if (lane == 0) __hip_atomic_fetch_add((LAS unsigned*)(L + L_FLAG + 4 * (i)), 1u, __ATOMIC_RELAXED, __HIP_MEMORY_SCOPE_WORKGROUP); CBAR(); } while (0)

__device__ __forceinline__ void dma4_kv(const void* sbase, unsigned v0, unsigned v1, unsigned v2, unsigned v3, unsigned dst) {
  unsigned keep;
  asm volatile(
      "s_mov_b32 %0, m0\n\ts_mov_b32 m0, %6\n\ts_nop 0\n\t"
      "global_load_lds_dwordx4 %1, %5 nt\n\t"
      "global_load_lds_dwordx4 %2, %5 offset:1024 nt\n\t"
      "global_load_lds_dwordx4 %3, %5 offset:2048 nt\n\t"
      "global_load_lds_dwordx4 %4, %5 offset:3072 nt\n\t"
      "s_mov_b32 m0, %0"
      : "=&s"(keep) : "v"(v0), "v"(v1), "v"(v2), "v"(v3), "s"(sbase), "s"(dst) : "memory");
}
#define DMA_STEP "global_load_lds_dwordx4 %1, %2\n\ts_add_u32 m0, m0, 0x400\n\tv_add_u32 %1, %4, %1\n\t"
template <int N> __device__ __forceinline__ void dma_w(const void* sbase, unsigned voff, unsigned step, unsigned dst) {
  unsigned keep;
  if constexpr (N == 4)
    asm volatile("s_mov_b32 %0, m0\n\ts_mov_b32 m0, %3\n\ts_nop 0\n\t" DMA_STEP DMA_STEP DMA_STEP DMA_STEP "s_mov_b32 m0, %0"
                 : "=&s"(keep), "+v"(voff) : "s"(sbase), "s"(dst), "s"(step) : "memory", "scc");
  else
    asm volatile("s_mov_b32 %0, m0\n\ts_mov_b32 m0, %3\n\ts_nop 0\n\t" DMA_STEP DMA_STEP "s_mov_b32 m0, %0"
                 : "=&s"(keep), "+v"(voff) : "s"(sbase), "s"(dst), "s"(step) : "memory", "scc");
}
__device__ __forceinline__ void dma1_w(const void* sbase, unsigned voff, unsigned dst) {
  unsigned keep;
  asm volatile("s_mov_b32 %0, m0\n\ts_mov_b32 m0, %3\n\ts_nop 0\n\tglobal_load_lds_dwordx4 %1, %2\n\ts_mov_b32 m0, %0"
               : "=&s"(keep) : "v"(voff), "s"(sbase), "s"(dst) : "memory");
}
__device__ __forceinline__ u32x2 tr_read(LAS unsigned char* L, unsigned a) {
  return __builtin_bit_cast(u32x2, __builtin_amdgcn_ds_read_tr16_b64_v4i16((LAS s16x4*)(L + a)));
}
__device__ __forceinline__ unsigned g_lo(float v, unsigned tagb) {
  unsigned u = __builtin_bit_cast(unsigned, v);
  u += 0x7fu + ((u >> 8) & 1u);
  return (u & 0xffffff00u) | tagb;
}
__device__ __forceinline__ float g_val(unsigned lo) { return __builtin_bit_cast(float, lo & 0xffffff00u); }
__device__ __forceinline__ unsigned g_bad(u32x4 v, unsigned taga, unsigned tagb) {
  return (v[1] ^ taga) | (v[3] ^ taga) | (((v[0] ^ tagb) | (v[2] ^ tagb)) & 0xffu);
}
__device__ __forceinline__ unsigned vprog(u32x4 v) { return min(min(v[0], v[1]), min(v[2], v[3])); }
constexpr int AUX_SC1 = 16;

#define PCHK(W, R0, R1, R2, R3, RR)                                    \
  "s_waitcnt vmcnt(" W ")\n\t"                                         \
  "v_xor_b32 %[t0], " R1 ", %[ta]\n\t"                                 \
  "v_xor_b32 %[t1], " R0 ", %[tb]\n\t"                                 \
  "v_xor_b32 %[t2], " R2 ", %[tb]\n\t"                                 \
  "v_or_b32 %[t1], %[t1], %[t2]\n\t"                                   \
  "v_xor_b32 %[t2], " R3 ", %[ta]\n\t"                                 \
  "v_and_b32 %[t1], 0xff, %[t1]\n\t"                                   \
  "v_or3_b32 %[t0], %[t0], %[t1], %[t2]\n\t"                           \
  "v_cmp_eq_u32 vcc, 0, %[t0]\n\t"                                     \
  "s_mov_b64 exec, vcc\n\t"                                            \
  "v_and_b32 %[r0], 0xffffff00, " R0 "\n\t"                            \
  "v_and_b32 %[r1], 0xffffff00, " R2 "\n\t"                            \
  "s_andn2_b64 %[pe], %[pe], vcc\n\t"                                  \
  "s_bcnt1_i32_b64 %[nb], %[pe]\n\t"                                   \
  "s_cmp_le_u32 %[nb], %[lf]\n\t"                                      \
  "s_cbranch_scc1 2f\n\t"                                              \
  "s_mov_b64 exec, %[pe]\n\t"                                          \
  "global_load_dwordx4 " RR ", %[vo], %[sb] sc1\n\t"
#define PA(W) PCHK(W, "v100", "v101", "v102", "v103", "v[100:103]")
#define PB(W) PCHK(W, "v104", "v105", "v106", "v107", "v[104:107]")
#define PC(W) PCHK(W, "v108", "v109", "v110", "v111", "v[108:111]")
#define PHEAD "s_mov_b64 %[sv], exec\n\ts_mov_b64 exec, %[pe]\n\ts_mov_b32 %[cn], 0x10000\n\tglobal_load_dwordx4 v[100:103], %[vo], %[sb] sc1\n\t"
#define PMORE(RR) "s_sleep %[sl]\n\tglobal_load_dwordx4 " RR ", %[vo], %[sb] sc1\n\t"
#define PTAIL "s_sub_u32 %[cn], %[cn], 1\n\ts_cbranch_scc0 1b\n\t2:\n\ts_mov_b64 exec, %[sv]\n\t"
#define POPS                                                                                                                                        \
  : [r0] "+v"(r0), [r1] "+v"(r1), [t0] "=&v"(t0), [t1] "=&v"(t1), [t2] "=&v"(t2), [sv] "=&s"(sv), [pe] "+s"(pend), [cn] "=&s"(cnt), [nb] "=&s"(nb) \
  : [vo] "v"(voff), [sb] "s"(sbase), [ta] "v"(taga), [tb] "v"(tagb), [lf] "s"(left), [sl] "i"(SLEEP)                                                \
  : "memory", "vcc", "scc", "v100", "v101", "v102", "v103", "v104", "v105", "v106", "v107", "v108", "v109", "v110", "v111"
template <int DEPTH, int SLEEP>
__device__ __forceinline__ u64 poll_pair(const void* sbase, unsigned voff, unsigned taga, unsigned tagb, float& r0, float& r1, u64 pend, unsigned left) {
  unsigned t0, t1, t2;
  u64 sv;
  unsigned cnt, nb;
  if constexpr (DEPTH == 1) asm volatile(PHEAD "1:\n\t" PA("0") PTAIL POPS);
  if constexpr (DEPTH == 2) asm volatile(PHEAD PMORE("v[104:107]") "1:\n\t" PA("1") PB("1") PTAIL POPS);
  if constexpr (DEPTH == 3) asm volatile(PHEAD PMORE("v[104:107]") PMORE("v[108:111]") "1:\n\t" PA("2") PB("2") PC("2") PTAIL POPS);
  return pend;
}

#define STAMP(i) do {} while (0)
#define STAMP_DECL() do {} while (0)
#define STAMP_FLUSH() do {} while (0)

__device__ __forceinline__ float gelu_tanh(float x) {
  const float u = 0.7978845608028654f * (x + 0.044715f * x * x * x);
  const float e = ex2(u * (2.0f * LOG2E));
  const float t = 1.0f - 2.0f * __builtin_amdgcn_rcpf(e + 1.0f);
  return 0.5f * x * (1.0f + t);
}

__global__ void __launch_bounds__(NTHR) __attribute__((amdgpu_num_vgpr(100)))
fused_kernel(const u16* __restrict__ x, const u16* __restrict__ kc, const u16* __restrict__ vc, const u16* __restrict__ wq,
             const u16* __restrict__ wo, const u16* __restrict__ w1, const u16* __restrict__ b1, const u16* __restrict__ w2,
             const u16* __restrict__ b2, u16* __restrict__ out, unsigned char* __restrict__ ws) {
  extern __shared__ __attribute__((aligned(16))) unsigned char smem[];
  LAS unsigned char* L = (LAS unsigned char*)smem;
  const unsigned lbase = (unsigned)(size_t)L;
  volatile LAS unsigned* flags = (volatile LAS unsigned*)(L + L_FLAG);
  const int tid = threadIdx.x, lane = tid & 63, wv = __builtin_amdgcn_readfirstlane(tid >> 6);
  const int g = blockIdx.x & 7, j = blockIdx.x >> 3;
  const int b = j >> 4, sp = j & 15, rb = j >> 1, ch = j & 1;
  const int lg = lane >> 4, lr = lane & 15;
  const int me = g * 32 + j;
  const int rch = me >> 7, ru = (me & 127) * 2 + (lane & 1);
  const int rcol = rch * 256 + (ru >> 5) * 32 + (ru & 15), rbt = (ru >> 4) & 1;
  const bool hasw1 = (j >> 2) != 3;
  const int p1 = hasw1 ? (j < 12 ? j : j - 4) * 8 + g : 0;
  const size_t kvoff = ((size_t)(b * HKV + g) * SK + sp * KEYS) * (DH * 2);
  const __amdgpu_buffer_rsrc_t rs = __builtin_amdgcn_make_buffer_rsrc((void*)ws, 0, (int)WS_END, 0x00020000);
  asm volatile("" ::"s"(x), "s"(kc), "s"(vc), "s"(wq), "s"(wo), "s"(w1), "s"(b1), "s"(w2), "s"(b2), "s"(out), "s"(ws));
  STAMP_DECL();
  STAMP(0);

  if (wv < 2) {
    __builtin_amdgcn_s_setprio(3);
    const bool isq = (wv == 0);
    const int rb2 = rb < 14 ? rb : 0;
    u32x4 xr0, xr1;
    u32x2 ep;
    unsigned bv;
    {
      const unsigned xo = lane * 32, zo = 0u, bo = (unsigned)((g * 224 + rb2 * 16 + 2 * (lane & 7)) * 2);
      asm volatile("global_load_dwordx4 %0, %4, %7\n\tglobal_load_dwordx4 %1, %4, %7 offset:16\n\t"
                   "global_load_dwordx2 %2, %5, %8 sc1\n\tglobal_load_dword %3, %6, %9"
                   : "=&v"(xr0), "=&v"(xr1), "=&v"(ep), "=&v"(bv) : "v"(xo), "v"(zo), "v"(bo), "s"(x), "s"(ws), "s"(b1) : "memory");
    }
    const int k0 = isq ? (j & 1) * 256 : (p1 & 3) * 128;
    const unsigned stg = isq ? L_WST : L_WST + 16384;
    if (isq && lane < 16) flags[lane] = 0u;
    BARRIER();
    STAMP(1);
    WAIT_VM(0);
    asm volatile("; PIN x" : "+v"(xr0), "+v"(xr1), "+v"(ep), "+v"(bv)::"memory");
    {
      float f[16];
#pragma unroll
      for (int i = 0; i < 4; ++i) { f[2 * i] = bflo(xr0[i]); f[2 * i + 1] = bfhi(xr0[i]); f[8 + 2 * i] = bflo(xr1[i]); f[9 + 2 * i] = bfhi(xr1[i]); }
      float s = 0.f;
#pragma unroll
      for (int i = 0; i < 16; ++i) s += f[i];
      const float mean = x16_sum(row_sum(s)) * (1.0f / DM);
      float q = 0.f;
#pragma unroll
      for (int i = 0; i < 16; ++i) { const float d = f[i] - mean; q += d * d; }
      const float var = x16_sum(row_sum(q)) * (1.0f / DM);
      const float mb = rbf(mean);
      const float inv = __builtin_amdgcn_rcpf(rbf(__builtin_amdgcn_sqrtf(rbf(rbf(var) + 1e-5f))));
      u32x4 o0, o1;
#pragma unroll
      for (int i = 0; i < 4; ++i) {
        const unsigned d0 = pack2(f[2 * i] - mb, f[2 * i + 1] - mb), d1 = pack2(f[8 + 2 * i] - mb, f[9 + 2 * i] - mb);
        o0[i] = pack2(bflo(d0) * inv, bfhi(d0) * inv);
        o1[i] = pack2(bflo(d1) * inv, bfhi(d1) * inv);
      }
      *(LAS u32x4*)(L + L_XN + wv * 2048 + lane * 32) = o0;
      *(LAS u32x4*)(L + L_XN + wv * 2048 + lane * 32 + 16) = o1;
    }
    const unsigned taga = ep[0] + 1u, tagb = (ep[1] + (taga == 0u ? 1u : 0u)) & 0xffu;
    STAMP(2);
    f32x4 acc[2] = {f32x4{0.f, 0.f, 0.f, 0.f}, f32x4{0.f, 0.f, 0.f, 0.f}};
    {
      unsigned ta = stg + (8 * lg + (lr >> 2)) * 64 + 8 * (lr & 3);
      unsigned xa = L_XN + wv * 2048 + (lr & 1) * 1024 + k0 * 2 + lg * 16;
      LDS_SPIN(flags[F_WIN] < 4u);
      _Pragma("nounroll") for (int hb = 0; hb < 2; ++hb) {
#pragma unroll
        for (int s = 0; s < 4; ++s) {
          const u32x4 xf = *(LAS u32x4*)(L + xa + s * 64);
#pragma unroll
          for (int t = 0; t < 2; ++t) {
            const u32x2 lo = tr_read(L, ta + s * 2048 + t * 32), hi = tr_read(L, ta + s * 2048 + t * 32 + 256);
            acc[t] = mfma32(u32x4{lo[0], lo[1], hi[0], hi[1]}, xf, acc[t]);
          }
        }
        if (!isq) break;
        ta += 8192; xa += 256;
      }
    }
    STAMP(3);
    if (lr < 8 && (isq || hasw1)) {
      const f32x4 a = (lr & 4) ? acc[1] : acc[0];
      const float v0 = (lr & 2) ? a[2] : a[0], v1 = (lr & 2) ? a[3] : a[1];
      const u32x4 gv = {g_lo(v0, tagb), taga, g_lo(v1, tagb), taga};
      const unsigned pb = isq ? WS_Q + (g * 2 + (j & 1)) * 4096 + (j >> 1) * 256 : WS_HP + (p1 >> 2) * 2048 + (p1 & 3) * 256;
      const unsigned sb = isq ? 65536u : 128u, st = isq ? 128u : 1024u;
      __builtin_amdgcn_raw_buffer_store_b128(gv, rs, (int)(pb + (lr & 1) * sb + (lr >> 2) * st + lg * 32 + (lr & 2) * 8), 0, AUX_SC1);
    }
    STAMP(4);
    __builtin_amdgcn_s_sleep(QSLEEP);
    u32x4 h0 = {0u, 0u, 0u, 0u};
    {
      const int qo = (int)(WS_Q + (b * 8 + g) * 8192) + wv * 2048 + lane * 16;
      const int ho = (int)(WS_HP + (g * 14 + rb2) * 1024) + lane * 16;
      u32x4 qv[4];
      bool ok[5];
#pragma unroll
      for (int i = 0; i < 4; ++i) { qv[i] = u32x4{0u, 0u, 0u, 0u}; ok[i] = false; }
      ok[4] = isq;
      _Pragma("nounroll") for (unsigned spins = 0; spins < AUX_CAP; ++spins) {
        CBAR();
#pragma unroll
        for (int i = 0; i < 4; ++i) if (!ok[i]) qv[i] = __builtin_amdgcn_raw_buffer_load_b128(rs, qo + (i & 1) * 1024 + (i >> 1) * 4096, 0, AUX_SC1);
        if (!ok[4]) h0 = __builtin_amdgcn_raw_buffer_load_b128(rs, ho, 0, AUX_SC1);
        bool all = true;
#pragma unroll
        for (int i = 0; i < 4; ++i) { ok[i] = g_bad(qv[i], taga, tagb) == 0u; all = all && ok[i]; }
        ok[4] = isq || g_bad(h0, taga, tagb) == 0u;
        if (__all(all && ok[4])) break;
      }
#pragma unroll
      for (int i = 0; i < 2; ++i)
        *(LAS unsigned*)(L + L_Q + wv * 512 + i * 256 + lane * 4) = pack2(g_val(qv[i][0]) + g_val(qv[2 + i][0]), g_val(qv[i][2]) + g_val(qv[2 + i][2]));
      if (isq && lane == 0) { flags[F_TAGA] = taga; flags[F_TAGB] = tagb; flags[F_EP0] = ep[0]; flags[F_EP1] = ep[1]; }
      LDS_INC(F_Q);
    }
    STAMP(5);
    if (!isq) {
      if (lane < 2) {
        const float xb0 = bf2f(x[rbt * DM + rcol]) + bf2f(b2[rcol]), xb1 = bf2f(x[rbt * DM + rcol + 16]) + bf2f(b2[rcol + 16]);
        *(LAS f32x2*)(L + L_XB + lane * 8) = f32x2{xb0, xb1};
      }
      const float a0 = xrow_sum(g_val(h0[0])), a1 = xrow_sum(g_val(h0[2]));
      const unsigned hv = rb < 14 ? pack2(gelu_tanh(rbf(rbf(a0) + bflo(bv))), gelu_tanh(rbf(rbf(a1) + bfhi(bv)))) : 0u;
      if (lane < 16) *(LAS unsigned*)(L + L_H + lane * 4) = hv;
      STAMP(6);
      STAMP_FLUSH();
      return;
    }
    LDS_SPIN(VPROG() < (unsigned)MLTRIG);
    STAMP(6);
    {
      float ms = 0.f, ls = 0.f;
      const unsigned mo = WS_ML + (g * 32 + (lane & 31)) * 128 + (rb >> 2) * 16;
      const u64 miss = poll_pair<1, 0>(ws, mo, taga, tagb, ms, ls, 0xffffffffull, (unsigned)H1LEFT);
      flags[F_GO1] = 1u;
      STAMP(7);
      if (miss) poll_pair<1, 0>(ws, mo, taga, tagb, ms, ls, miss, 0u);
      const float e = ex2((ms - row_max(ms)) * LOG2E);
      const float w = e * __builtin_amdgcn_rcpf(row_sum(e * ls));
      if (lane < 32) *(LAS float*)(L + L_W + lane * 4) = w;
      STAMP(8);
      BARRIER();
      poll_pair<1, 0>(ws, WS_H2 + (rch * 128 + (lane & 7) * 16 + (j & 15)) * 4096 + ru * 16, taga, tagb, ms, ls, 0xffull, (unsigned)H2LEFT);
      flags[F_GO2] = 1u;
    }
    STAMP(9);
    STAMP_FLUSH();
    return;
  }

  if (wv >= NAUX + NSTR) {
    const int ld = wv - (NAUX + NSTR);
    volatile LAS unsigned* prog = flags + F_VP + ld;
    __builtin_amdgcn_s_setprio(3);
    dma_w<4>((const unsigned char*)(wq + (size_t)((j & 1) * 256 + ld * 64) * (HQ * DH) + g * 512 + (j >> 1) * 32), (lane >> 2) * (HQ * DH * 2) + (lane & 3) * 16,
             16 * HQ * DH * 2, lbase + L_WST + ld * 4096);
    if (hasw1)
      dma_w<2>((const unsigned char*)(w1 + (size_t)((p1 & 3) * 128 + ld * 32) * FF + (p1 >> 2) * 32), (lane >> 2) * (FF * 2) + (lane & 3) * 16, 16 * FF * 2,
               lbase + L_WST + 16384 + ld * 2048);
    __builtin_amdgcn_s_setprio(0);
    BARRIER();
    WAIT_VM(0);
    LDS_INC(F_WIN);
    STAMP(1);
    const unsigned char* vbase = (const unsigned char*)vc + kvoff + ld * 4096;
    const unsigned dst = lbase + L_V + ld * 4096;
    const unsigned ve = lg * 256 + ((((lr >> 1) ^ lg) << 1) | (lr & 1)) * 16, vo = ve ^ 128u;
    _Pragma("nounroll") for (int c = 0; c < 9; ++c) {
      if (c == WPOS) {
        LDS_SPIN(flags[F_Q] < 2u);
        STAMP(2);
        const int hl = lane >> 5, slot = lane & 31, rb2 = rb < 14 ? rb : 0;
        const unsigned char* wob = (const unsigned char*)(wo + (size_t)(g * 512 + rb * 32 + ld * 8) * DM + ch * 256);
        const unsigned char* w2b = (const unsigned char*)(w2 + (size_t)(g * 224 + rb2 * 16 + ld * 4) * DM + ch * 256);
        const unsigned base = hl * 1024 + slot * 16;
        _Pragma("nounroll") for (int i = 0; i < 4; ++i)
          dma1_w(wob + i * 2048, base ^ ((((ld & 1) << 2) | ((i & 1) << 1) | hl) << 5), lbase + L_WST + ld * 4096 + i * 1024);
        _Pragma("nounroll") for (int i = 0; i < 2; ++i)
          dma1_w(w2b + i * 2048, base ^ (((4 * ld + 2 * i + hl) & 7) << 5), lbase + L_WST + 16384 + ld * 2048 + i * 1024);
      }
      if (c < 8) dma4_kv(vbase + c * 16384, ve, vo, ve, vo, dst + c * 16384);
    }
    STAMP(4);
    static_assert(WPOS >= 4 && WPOS <= 8, "the counts below");
    WAIT_VM(22); *prog = 4u;
    if (WPOS >= 6) WAIT_VM(14); else WAIT_VM(8);
    *prog = 6u;
    if (WPOS >= 7) WAIT_VM(10); else WAIT_VM(4);
    *prog = 7u;
    if (WPOS >= 8) WAIT_VM(6); else WAIT_VM(0);
    *prog = 8u;
    WAIT_VM(0);
    STAMP(5);
    STAMP_FLUSH();
    return;
  }

  const int sw = wv - NAUX;
  __builtin_amdgcn_s_setprio(2);
  u32x4 kf[4][4];
  {
    const unsigned char* kbase = (const unsigned char*)kc + kvoff + sw * 4096;
    const unsigned ko = lr * 256 + lg * 16;
#pragma unroll
    for (int t = 0; t < 4; ++t) {
      if (t == INITK) BARRIER();
      asm volatile("global_load_dwordx4 %0, %4, %5 nt\n\tglobal_load_dwordx4 %1, %4, %5 offset:64 nt\n\t"
                   "global_load_dwordx4 %2, %4, %5 offset:128 nt\n\tglobal_load_dwordx4 %3, %4, %5 offset:192 nt"
                   : "=&v"(kf[t][0]), "=&v"(kf[t][1]), "=&v"(kf[t][2]), "=&v"(kf[t][3]) : "v"(ko), "s"(kbase + t * 32768) : "memory");
    }
  }
  STAMP(1);
  LDS_SPIN(flags[F_Q] < 2u);
  u32x4 qf[4];
#pragma unroll
  for (int s = 0; s < 4; ++s) qf[s] = *(LAS u32x4*)(L + L_Q + (lr & 3) * 256 + s * 64 + lg * 16);
  const unsigned taga = flags[F_TAGA], tagb = flags[F_TAGB];
  asm volatile("s_waitcnt vmcnt(0)"
               : "+v"(kf[0][0]), "+v"(kf[0][1]), "+v"(kf[0][2]), "+v"(kf[0][3]), "+v"(kf[1][0]), "+v"(kf[1][1]), "+v"(kf[1][2]), "+v"(kf[1][3]),
                 "+v"(kf[2][0]), "+v"(kf[2][1]), "+v"(kf[2][2]), "+v"(kf[2][3]), "+v"(kf[3][0]), "+v"(kf[3][1]), "+v"(kf[3][2]), "+v"(kf[3][3])
               :: "memory");
  STAMP(2);
  float mcu;
  {
    float sc[4][4];
    float m = -INFINITY;
#pragma unroll
    for (int t = 0; t < 4; ++t) {
      f32x4 a = {0.f, 0.f, 0.f, 0.f};
#pragma unroll
      for (int s = 0; s < 4; ++s) a = mfma32(kf[t][s], qf[s], a);
      const unsigned r01 = pack2(a[0], a[1]), r23 = pack2(a[2], a[3]);
      const unsigned s01 = pack2(bflo(r01) * SCALE, bfhi(r01) * SCALE), s23 = pack2(bflo(r23) * SCALE, bfhi(r23) * SCALE);
      sc[t][0] = bflo(s01); sc[t][1] = bfhi(s01); sc[t][2] = bflo(s23); sc[t][3] = bfhi(s23);
      m = fmaxf(fmaxf(m, sc[t][0]), fmaxf(sc[t][1], fmaxf(sc[t][2], sc[t][3])));
    }
    m = xrow_max(m);
    if (lane < 4) *(LAS float*)(L + L_M + lane * 32 + sw * 4) = m;
    LDS_INC(F_MAX);
    LDS_SPIN_HOT(flags[F_MAX] < 8u);
    {
      const f32x4 m0 = *(LAS f32x4*)(L + L_M + (lr & 3) * 32), m1 = *(LAS f32x4*)(L + L_M + (lr & 3) * 32 + 16);
      m = fmaxf(fmaxf(fmaxf(m0[0], m0[1]), fmaxf(m0[2], m0[3])), fmaxf(fmaxf(m1[0], m1[1]), fmaxf(m1[2], m1[3])));
    }
    mcu = m;
    float l = 0.f;
#pragma unroll
    for (int t = 0; t < 4; ++t) {
#pragma unroll
      for (int r = 0; r < 4; ++r) { sc[t][r] = ex2((sc[t][r] - m) * LOG2E); l += sc[t][r]; }
      if (lr < 4) *(LAS u32x2*)(L + L_P + (4 * t + (sw >> 1)) * 256 + lr * 64 + lg * 16 + (sw & 1) * 8) = u32x2{pack2(sc[t][0], sc[t][1]), pack2(sc[t][2], sc[t][3])};
    }
    l = xrow_sum(l);
    if (lane < 4) *(LAS float*)(L + L_L + lane * 32 + sw * 4) = l;
    LDS_INC(F_P);
  }
  STAMP(3);
  LDS_SPIN_HOT(flags[F_P] < 8u);
  u32x4 gml;
  {
    const f32x4 l0 = *(LAS f32x4*)(L + L_L + (lr & 3) * 32), l1 = *(LAS f32x4*)(L + L_L + (lr & 3) * 32 + 16);
    gml = u32x4{g_lo(mcu, tagb), taga, g_lo(((l0[0] + l0[1]) + (l0[2] + l0[3])) + ((l1[0] + l1[1]) + (l1[2] + l1[3])), tagb), taga};
    asm volatile("" : "+v"(gml));
  }
  f32x4 o = {0.f, 0.f, 0.f, 0.f};
  {
    u32x4 pf[16];
#pragma unroll
    for (int s = 0; s < 16; ++s) pf[s] = *(LAS u32x4*)(L + L_P + s * 256 + (lr & 3) * 64 + lg * 16);
    const unsigned tb = L_V + (4 * lg + (lr >> 2)) * 256 + ((sw ^ (4 * (lg & 1) + (lr >> 2))) << 5) + 8 * (lr & 3);
#pragma unroll
    for (int s = 0; s < 16; ++s) {
      if (s == 0) { LDS_SPIN(VPROG() < 4u); STAMP(4); }
      if (s == 8) LDS_SPIN(VPROG() < 6u);
      if (s == 12) LDS_SPIN_HOT(VPROG() < 7u);
      if (s == 14) { LDS_SPIN_HOT(VPROG() < 8u); STAMP(5); }
      const u32x2 lo = tr_read(L, tb + s * 8192), hi = tr_read(L, tb + s * 8192 + 4096);
      o = mfma32(u32x4{lo[0], lo[1], hi[0], hi[1]}, pf[s], o);
      if (s == 7 || s == 11 || s == 13) SCHED();
    }
  }
  if (lr < 8) {
    const float v0 = (lr & 4) ? o[2] : o[0], v1 = (lr & 4) ? o[3] : o[1];
    const u32x4 gv = {g_lo(v0, tagb), taga, g_lo(v1, tagb), taga};
    __builtin_amdgcn_raw_buffer_store_b128(gv, rs, (int)(WS_H1 + me * 4096 + sw * 128) + (lr & 3) * 1024 + lg * 32 + (lr & 4) * 4, 0, AUX_SC1);
  }
  if (sw == 0 && lane < 4) __builtin_amdgcn_raw_buffer_store_b128(gml, rs, (int)(WS_ML + me * 128) + lane * 16, 0, AUX_SC1);
  STAMP(7);
  {
    const unsigned oo = WS_H1 + ((g * 32 + sw * 4 + lg) * 4 + (rb >> 2)) * 1024 + (rb & 3) * 256 + lr * 16;
    float v0 = 0.f, v1 = 0.f;
    LDS_SPIN_HOT(flags[F_GO1] == 0u);
    poll_pair<PDEPTH, PSLEEP>(ws, oo, taga, tagb, v0, v1, ~0ull, 0u);
    STAMP(8);
    *(LAS unsigned*)(L + L_OX + (sw * 4 + lg) * 64 + lr * 4) = pack2(v0, v1);
  }
  BARRIER();
  {
    const int q = lr >> 2;
    const unsigned ta = L_WST + (8 * lg + q) * 512 + (((2 * sw) ^ (q | ((lg & 1) << 2))) << 5) + 8 * (lr & 3);
    const unsigned t2 = L_WST + 16384 + (4 * lg + q) * 512 + (((2 * sw) ^ ((4 * lg + q) & 7)) << 5) + 8 * (lr & 3);
    const u32x4 of0 = *(LAS u32x4*)(L + L_OX + lr * 64 + lg * 16), of1 = *(LAS u32x4*)(L + L_OX + 1024 + lr * 64 + lg * 16);
    const f32x4 w0 = *(LAS f32x4*)(L + L_W + lg * 16), w1v = *(LAS f32x4*)(L + L_W + 64 + lg * 16);
    const u32x2 hf = *(LAS u32x2*)(L + L_H + (lr & 1) * 32 + lg * 8);
    float r[2][2];
#pragma unroll
    for (int t = 0; t < 2; ++t) {
      const u32x2 lo = tr_read(L, ta ^ (t << 5)), hi = tr_read(L, (ta ^ (t << 5)) + 2048);
      const u32x2 w2f = tr_read(L, t2 ^ (t << 5));
      const u32x4 wf = {lo[0], lo[1], hi[0], hi[1]};
      const f32x4 a0 = mfma32(of0, wf, f32x4{0.f, 0.f, 0.f, 0.f}), a1 = mfma32(of1, wf, f32x4{0.f, 0.f, 0.f, 0.f});
      const f32x4 mac = mfma16(hf, w2f, f32x4{0.f, 0.f, 0.f, 0.f});
      const float p0 = (a0[0] * w0[0] + a0[1] * w0[1]) + (a0[2] * w0[2] + a0[3] * w0[3]);
      const float p1v = (a1[0] * w1v[0] + a1[1] * w1v[1]) + (a1[2] * w1v[2] + a1[3] * w1v[3]);
      auto sx = __builtin_amdgcn_permlane16_swap(__float_as_uint(p0), __float_as_uint(p1v), false, false);
      r[t][0] = x32_sum(__uint_as_float(sx[0]) + __uint_as_float(sx[1]));
      r[t][1] = (lg & 1) ? mac[1] : mac[0];
    }
    if (lane < 32) {
      const u32x4 gv = {g_lo(r[0][0] + r[0][1], tagb), taga, g_lo(r[1][0] + r[1][1], tagb), taga};
      __builtin_amdgcn_raw_buffer_store_b128(gv, rs, (int)(WS_H2 + ((ch * 128 + g * 16 + rb) * 8 + sw) * 512) + lane * 16, 0, AUX_SC1);
    }
  }
  STAMP(9);
  if (sw < 4) {
    const unsigned ho = WS_H2 + (rch * 128 + sw * 32 + (lane >> 1)) * 4096 + ru * 16;
    float s0 = 0.f, s1 = 0.f;
    LDS_SPIN_HOT(flags[F_GO2] == 0u);
    poll_pair<PDEPTH, PSLEEP>(ws, ho, taga, tagb, s0, s1, ~0ull, 0u);
    STAMP(10);
    s0 += dpp<XOR2>(s0); s1 += dpp<XOR2>(s1);
    s0 += dpp<ROR4>(s0); s1 += dpp<ROR4>(s1);
    s0 += dpp<ROR8>(s0); s1 += dpp<ROR8>(s1);
    s0 = xrow_sum(s0); s1 = xrow_sum(s1);
    if (lane < 2) *(LAS f32x2*)(L + L_R + sw * 16 + lane * 8) = f32x2{s0, s1};
    CBAR();
    unsigned old = 0u;
    if (lane == 0) old = __hip_atomic_fetch_add((LAS unsigned*)(L + L_FLAG + 4 * F_RED), 1u, __ATOMIC_RELAXED, __HIP_MEMORY_SCOPE_WORKGROUP);
    CBAR();
    if (__builtin_amdgcn_readfirstlane(old) == 3u) {
      if (lane < 2) {
        f32x2 r = *(LAS f32x2*)(L + L_XB + lane * 8);
#pragma unroll
        for (int k = 0; k < 4; ++k) r += *(LAS f32x2*)(L + L_R + k * 16 + lane * 8);
        out[rbt * DM + rcol] = (u16)f2bf(r[0]);
        out[rbt * DM + rcol + 16] = (u16)f2bf(r[1]);
      }
      if (me == 0 && lane == 0) {
        const u64 e = (((u64)flags[F_EP1] << 32) | flags[F_EP0]) + 1ull;
        __hip_atomic_store((GAS u64*)(ws + WS_EPOCH), e, __ATOMIC_RELAXED, __HIP_MEMORY_SCOPE_AGENT);
      }
      STAMP(11);
    }
  }
  STAMP_FLUSH();
}

extern "C" void kernel_launch(void* const* d_in, const int* in_sizes, int n_in, void* d_out, int out_size, void* d_ws, size_t ws_size,
                              hipStream_t stream) {
  (void)hipFuncSetAttribute((const void*)fused_kernel, hipFuncAttributeMaxDynamicSharedMemorySize, L_BYTES);
  fused_kernel<<<256, NTHR, L_BYTES, stream>>>((const u16*)d_in[0], (const u16*)d_in[1], (const u16*)d_in[2], (const u16*)d_in[3],
                                               (const u16*)d_in[4], (const u16*)d_in[6], (const u16*)d_in[7], (const u16*)d_in[8],
                                               (const u16*)d_in[9], (u16*)d_out, (unsigned char*)d_ws);
}
```

```cpp
#include <hip/hip_runtime.h>
#include <hip/hip_bf16.h>
#include <stdint.h>

#define GAS __attribute__((address_space(1)))
#define LAS __attribute__((address_space(3)))
typedef unsigned u32x4 __attribute__((ext_vector_type(4)));
typedef unsigned u32x2 __attribute__((ext_vector_type(2)));
typedef float f32x4 __attribute__((ext_vector_type(4)));
typedef float f32x2 __attribute__((ext_vector_type(2)));
typedef short s16x4 __attribute__((ext_vector_type(4)));
typedef __bf16 bf16x8 __attribute__((ext_vector_type(8)));
typedef unsigned short u16;
typedef unsigned long long u64;

#define WLATE 4
#define H2LEFT 1
#define MLDEPTH 1
#define MLSLEEP 2
#define INITK 1
#define FIN1 1
#define MLPOS 14
#define TG 8
#define TSKEW 32
#define KVNT 0
#define NTS ""
#define KDELAY 0
#define VGATE 8
#define QGATE 8
#define QWAIT 12
#define QSLEEP 24
#define MLTRIG 8
#define H1LEFT 3
#define PDEPTH 1
#define PSLEEP 4

constexpr int DM = 512, HQ = 32, HKV = 8, SK = 8192, DH = 128, FF = 1792;
constexpr int KEYS = 512;
constexpr int NAUX = 2, NSTR = 8, NLD = 4, NTHR = (NAUX + NSTR + NLD) * 64;
constexpr float SCALE = 0.08838834764831845f, LOG2E = 1.4426950408889634f;
constexpr unsigned SPIN_CAP = 1u << 17;
constexpr unsigned AUX_CAP = SPIN_CAP;
#define SPIN_SLEEP 1

constexpr unsigned WS_EPOCH = 0;
constexpr unsigned WS_ML = 393216;
constexpr unsigned WS_Q = 32768;
constexpr unsigned WS_HP = WS_Q + 131072;
constexpr unsigned WS_H1 = 524288;
constexpr unsigned WS_H2 = WS_H1 + (1u << 20);
constexpr unsigned WS_END = WS_H2 + (1u << 20);
constexpr unsigned WS_STAMP = 8u << 20;

constexpr unsigned L_V = 0;
constexpr unsigned L_WST = 131072;
constexpr unsigned L_XN = L_WST + 24576;
constexpr unsigned L_P = L_XN;
constexpr unsigned L_Q = L_XN + 4096;
constexpr unsigned L_OX = L_Q + 1024;
constexpr unsigned L_M = L_OX + 2048;
constexpr unsigned L_L = L_M + 128;
constexpr unsigned L_W = L_L + 128;
constexpr unsigned L_H = L_W + 128;
constexpr unsigned L_XB = L_H + 64;
constexpr unsigned L_Z = L_XB + 32;
constexpr unsigned L_FLAG = L_Z + 256;
constexpr unsigned L_BYTES = L_FLAG + 64;
static_assert(L_BYTES <= 160 * 1024, "LDS");
enum { F_VP = 0, F_Q = 4, F_MAX = 5, F_P = 6, F_KISS = 7, F_QISS = 12, F_GO1 = 14, F_GO2 = 15, F_WIN = 13, F_TAGA = 8, F_TAGB = 9, F_EP0 = 10, F_EP1 = 11 };

__device__ __forceinline__ float bf2f(unsigned b) { return __builtin_bit_cast(float, b << 16); }
__device__ __forceinline__ float bflo(unsigned u) { return __builtin_bit_cast(float, u << 16); }
__device__ __forceinline__ float bfhi(unsigned u) { return __builtin_bit_cast(float, u & 0xffff0000u); }
__device__ __forceinline__ unsigned f2bf(float f) { return (unsigned)__builtin_bit_cast(u16, (__bf16)f); }
__device__ __forceinline__ float rbf(float f) { return bf2f(f2bf(f)); }
__device__ __forceinline__ unsigned pack2(float lo, float hi) {
  typedef __bf16 bf16x2 __attribute__((ext_vector_type(2)));
  const f32x2 v = {lo, hi};
  return __builtin_bit_cast(unsigned, __builtin_convertvector(v, bf16x2));
}
__device__ __forceinline__ f32x4 mfma32(u32x4 a, u32x4 b, f32x4 c) {
  return __builtin_amdgcn_mfma_f32_16x16x32_bf16(__builtin_bit_cast(bf16x8, a), __builtin_bit_cast(bf16x8, b), c, 0, 0, 0);
}
__device__ __forceinline__ f32x4 mfma16(u32x2 a, u32x2 b, f32x4 c) {
  return __builtin_amdgcn_mfma_f32_16x16x16bf16_1k(__builtin_bit_cast(s16x4, a), __builtin_bit_cast(s16x4, b), c, 0, 0, 0);
}
template <int CTRL> __device__ __forceinline__ float dpp(float x) {
  return __builtin_bit_cast(float, __builtin_amdgcn_mov_dpp(__builtin_bit_cast(int, x), CTRL, 0xf, 0xf, true));
}
constexpr int XOR1 = 0xB1, XOR2 = 0x4E, HMIR = 0x141, MIR = 0x140, ROR4 = 0x124, ROR8 = 0x128;
__device__ __forceinline__ float row_sum(float v) { v += dpp<XOR1>(v); v += dpp<XOR2>(v); v += dpp<HMIR>(v); v += dpp<MIR>(v); return v; }
__device__ __forceinline__ float row_max(float v) {
  v = fmaxf(v, dpp<XOR1>(v)); v = fmaxf(v, dpp<XOR2>(v)); v = fmaxf(v, dpp<HMIR>(v)); v = fmaxf(v, dpp<MIR>(v)); return v;
}
__device__ __forceinline__ float x16_sum(float x) {
  auto s = __builtin_amdgcn_permlane16_swap(__float_as_uint(x), __float_as_uint(x), false, false);
  return __uint_as_float(s[0]) + __uint_as_float(s[1]);
}
__device__ __forceinline__ float x32_sum(float x) {
  auto t = __builtin_amdgcn_permlane32_swap(__float_as_uint(x), __float_as_uint(x), false, false);
  return __uint_as_float(t[0]) + __uint_as_float(t[1]);
}
__device__ __forceinline__ float xrow_sum(float x) { return x32_sum(x16_sum(x)); }
__device__ __forceinline__ float xrow_max(float x) {
  auto s = __builtin_amdgcn_permlane16_swap(__float_as_uint(x), __float_as_uint(x), false, false);
  x = fmaxf(__uint_as_float(s[0]), __uint_as_float(s[1]));
  auto t = __builtin_amdgcn_permlane32_swap(__float_as_uint(x), __float_as_uint(x), false, false);
  return fmaxf(__uint_as_float(t[0]), __uint_as_float(t[1]));
}
__device__ __forceinline__ float ex2(float x) { return __builtin_amdgcn_exp2f(x); }

#define STR_(x) #x
#define STR(x) STR_(x)
#define CBAR() asm volatile("" ::: "memory")
#define SCHED() __builtin_amdgcn_sched_barrier(0)
#define WAIT_VM(n) asm volatile("s_waitcnt vmcnt(" #n ")" ::: "memory")
#define BARRIER() do { asm volatile("s_waitcnt lgkmcnt(0)" ::: "memory"); __builtin_amdgcn_s_barrier(); CBAR(); } while (0)
#define LDS_SPIN(cond) do { _Pragma("nounroll") for (unsigned spins_ = 0; spins_ < (SPIN_CAP << 4) && (cond); ++spins_) __builtin_amdgcn_s_sleep(SPIN_SLEEP); CBAR(); } while (0)
#define LDS_SPIN_HOT(cond) do { _Pragma("nounroll") for (unsigned spins_ = 0; spins_ < (SPIN_CAP << 5) && (cond); ++spins_) {} CBAR(); } while (0)
#define VPROG() vprog(*(volatile LAS u32x4*)(L + L_FLAG))
#define VWAIT(n) do { _Pragma("nounroll") for (unsigned spins_ = 0; spins_ < (SPIN_CAP << 5) && vp < (n); ++spins_) vp = __builtin_amdgcn_readfirstlane(VPROG()); CBAR(); } while (0)
#define LDS_INC(i) do { CBAR(); if (lane == 0) __hip_atomic_fetch_add((LAS unsigned*)(L + L_FLAG + 4 * (i)), 1u, __ATOMIC_RELAXED, __HIP_MEMORY_SCOPE_WORKGROUP); CBAR(); } while (0)

__device__ __forceinline__ void dma4_kv(const void* sbase, unsigned v0, unsigned v1, unsigned v2, unsigned v3, unsigned dst) {
  unsigned keep;
  asm volatile(
      "s_mov_b32 %0, m0\n\ts_mov_b32 m0, %6\n\ts_nop 0\n\t"
      "global_load_lds_dwordx4 %1, %5" NTS "\n\t"
      "global_load_lds_dwordx4 %2, %5 offset:1024" NTS "\n\t"
      "global_load_lds_dwordx4 %3, %5 offset:2048" NTS "\n\t"
      "global_load_lds_dwordx4 %4, %5 offset:3072" NTS "\n\t"
      "s_mov_b32 m0, %0"
      : "=&s"(keep) : "v"(v0), "v"(v1), "v"(v2), "v"(v3), "s"(sbase), "s"(dst) : "memory");
}
#define DMA_STEP "global_load_lds_dwordx4 %1, %2\n\ts_add_u32 m0, m0, 0x400\n\tv_add_u32 %1, %4, %1\n\t"
template <int N> __device__ __forceinline__ void dma_w(const void* sbase, unsigned voff, unsigned step, unsigned dst) {
  unsigned keep;
  if constexpr (N == 4)
    asm volatile("s_mov_b32 %0, m0\n\ts_mov_b32 m0, %3\n\ts_nop 0\n\t" DMA_STEP DMA_STEP DMA_STEP DMA_STEP "s_mov_b32 m0, %0"
                 : "=&s"(keep), "+v"(voff) : "s"(sbase), "s"(dst), "s"(step) : "memory", "scc");
  else
    asm volatile("s_mov_b32 %0, m0\n\ts_mov_b32 m0, %3\n\ts_nop 0\n\t" DMA_STEP DMA_STEP "s_mov_b32 m0, %0"
                 : "=&s"(keep), "+v"(voff) : "s"(sbase), "s"(dst), "s"(step) : "memory", "scc");
}
__device__ __forceinline__ void dma1_w(const void* sbase, unsigned voff, unsigned dst) {
  unsigned keep;
  asm volatile("s_mov_b32 %0, m0\n\ts_mov_b32 m0, %3\n\ts_nop 0\n\tglobal_load_lds_dwordx4 %1, %2\n\ts_mov_b32 m0, %0"
               : "=&s"(keep) : "v"(voff), "s"(sbase), "s"(dst) : "memory");
}
__device__ __forceinline__ u32x2 tr_read(LAS unsigned char* L, unsigned a) {
  return __builtin_bit_cast(u32x2, __builtin_amdgcn_ds_read_tr16_b64_v4i16((LAS s16x4*)(L + a)));
}
__device__ __forceinline__ unsigned g_lo(float v, unsigned tagb) {
  unsigned u = __builtin_bit_cast(unsigned, v);
  u += 0x7fu + ((u >> 8) & 1u);
  return (u & 0xffffff00u) | tagb;
}
__device__ __forceinline__ float g_val(unsigned lo) { return __builtin_bit_cast(float, lo & 0xffffff00u); }
__device__ __forceinline__ unsigned g_bad(u32x4 v, unsigned taga, unsigned tagb) {
  return (v[1] ^ taga) | (v[3] ^ taga) | (((v[0] ^ tagb) | (v[2] ^ tagb)) & 0xffu);
}
__device__ __forceinline__ unsigned vprog(u32x4 v) { return min(min(v[0], v[1]), min(v[2], v[3])); }
constexpr int AUX_SC1 = 16;

#define PCHK(W, R0, R1, R2, R3, RR)                                    \
  "s_waitcnt vmcnt(" W ")\n\t"                                         \
  "v_xor_b32 %[t0], " R1 ", %[ta]\n\t"                                 \
  "v_xor_b32 %[t1], " R0 ", %[tb]\n\t"                                 \
  "v_xor_b32 %[t2], " R2 ", %[tb]\n\t"                                 \
  "v_or_b32 %[t1], %[t1], %[t2]\n\t"                                   \
  "v_xor_b32 %[t2], " R3 ", %[ta]\n\t"                                 \
  "v_and_b32 %[t1], 0xff, %[t1]\n\t"                                   \
  "v_or3_b32 %[t0], %[t0], %[t1], %[t2]\n\t"                           \
  "v_cmp_eq_u32 vcc, 0, %[t0]\n\t"                                     \
  "s_mov_b64 exec, vcc\n\t"                                            \
  "v_and_b32 %[r0], 0xffffff00, " R0 "\n\t"                            \
  "v_and_b32 %[r1], 0xffffff00, " R2 "\n\t"                            \
  "s_andn2_b64 %[pe], %[pe], vcc\n\t"                                  \
  "s_bcnt1_i32_b64 %[nb], %[pe]\n\t"                                   \
  "s_cmp_le_u32 %[nb], %[lf]\n\t"                                      \
  "s_cbranch_scc1 2f\n\t"                                              \
  "s_mov_b64 exec, %[pe]\n\t"                                          \
  "global_load_dwordx4 " RR ", %[vo], %[sb] sc1\n\t"
#define PA(W) PCHK(W, "v100", "v101", "v102", "v103", "v[100:103]")
#define PB(W) PCHK(W, "v104", "v105", "v106", "v107", "v[104:107]")
#define PC(W) PCHK(W, "v108", "v109", "v110", "v111", "v[108:111]")
#define PHEAD "s_mov_b64 %[sv], exec\n\ts_mov_b64 exec, %[pe]\n\ts_mov_b32 %[cn], 0x10000\n\tglobal_load_dwordx4 v[100:103], %[vo], %[sb] sc1\n\t"
#define PMORE(RR) "s_sleep %[sl]\n\tglobal_load_dwordx4 " RR ", %[vo], %[sb] sc1\n\t"
#define PTAIL "s_sub_u32 %[cn], %[cn], 1\n\ts_cbranch_scc0 1b\n\t2:\n\ts_mov_b64 exec, %[sv]\n\t"
#define POPS                                                                                                                                        \
  : [r0] "+v"(r0), [r1] "+v"(r1), [t0] "=&v"(t0), [t1] "=&v"(t1), [t2] "=&v"(t2), [sv] "=&s"(sv), [pe] "+s"(pend), [cn] "=&s"(cnt), [nb] "=&s"(nb) \
  : [vo] "v"(voff), [sb] "s"(sbase), [ta] "v"(taga), [tb] "v"(tagb), [lf] "s"(left), [sl] "i"(SLEEP)                                                \
  : "memory", "vcc", "scc", "v100", "v101", "v102", "v103", "v104", "v105", "v106", "v107", "v108", "v109", "v110", "v111"
template <int DEPTH, int SLEEP>
__device__ __forceinline__ u64 poll_pair(const void* sbase, unsigned voff, unsigned taga, unsigned tagb, float& r0, float& r1, u64 pend, unsigned left) {
  unsigned t0, t1, t2;
  u64 sv;
  unsigned cnt, nb;
  if constexpr (DEPTH == 1) asm volatile(PHEAD "1:\n\t" PA("0") PTAIL POPS);
  if constexpr (DEPTH == 2) asm volatile(PHEAD PMORE("v[104:107]") "1:\n\t" PA("1") PB("1") PTAIL POPS);
  if constexpr (DEPTH == 3) asm volatile(PHEAD PMORE("v[104:107]") PMORE("v[108:111]") "1:\n\t" PA("2") PB("2") PC("2") PTAIL POPS);
  return pend;
}

#define QLOAD "global_load_dwordx4 v[100:103], %[vo], %[sb] sc1\n\tglobal_load_dwordx4 v[104:107], %[v1], %[sb] sc1\n\t" \
              "global_load_dwordx4 v[108:111], %[v2], %[sb] sc1\n\tglobal_load_dwordx4 v[112:115], %[v3], %[sb] sc1\n\t"
__device__ __forceinline__ void poll_quad(const void* sbase, unsigned voff, unsigned taga, unsigned tagb, float& r0, float& r1) {
  unsigned t0, t1, t2;
  u64 sv, pend;
  unsigned cnt;
  asm volatile(
      "s_mov_b64 %[sv], exec\n\ts_mov_b64 %[pe], exec\n\ts_mov_b32 %[cn], 0x10000\n\t"
      "1:\n\t" QLOAD
      "s_waitcnt vmcnt(0)\n\t"
      "v_xor_b32 %[t0], v101, %[ta]\n\tv_xor_b32 %[t1], v100, %[tb]\n\t"
      "v_bitop3_b32 %[t0], v103, %[ta], %[t0] bitop3:0xbe\n\tv_bitop3_b32 %[t1], v102, %[tb], %[t1] bitop3:0xbe\n\t"
      "v_bitop3_b32 %[t0], v105, %[ta], %[t0] bitop3:0xbe\n\tv_bitop3_b32 %[t1], v104, %[tb], %[t1] bitop3:0xbe\n\t"
      "v_bitop3_b32 %[t0], v107, %[ta], %[t0] bitop3:0xbe\n\tv_bitop3_b32 %[t1], v106, %[tb], %[t1] bitop3:0xbe\n\t"
      "v_bitop3_b32 %[t0], v109, %[ta], %[t0] bitop3:0xbe\n\tv_bitop3_b32 %[t1], v108, %[tb], %[t1] bitop3:0xbe\n\t"
      "v_bitop3_b32 %[t0], v111, %[ta], %[t0] bitop3:0xbe\n\tv_bitop3_b32 %[t1], v110, %[tb], %[t1] bitop3:0xbe\n\t"
      "v_bitop3_b32 %[t0], v113, %[ta], %[t0] bitop3:0xbe\n\tv_bitop3_b32 %[t1], v112, %[tb], %[t1] bitop3:0xbe\n\t"
      "v_bitop3_b32 %[t0], v115, %[ta], %[t0] bitop3:0xbe\n\tv_bitop3_b32 %[t1], v114, %[tb], %[t1] bitop3:0xbe\n\t"
      "v_and_b32 %[t1], 0xff, %[t1]\n\t"
      "v_or_b32 %[t0], %[t0], %[t1]\n\t"
      "v_cmp_eq_u32 vcc, 0, %[t0]\n\t"
      "s_mov_b64 exec, vcc\n\t"
      "v_and_b32 v100, 0xffffff00, v100\n\tv_and_b32 v102, 0xffffff00, v102\n\tv_and_b32 v104, 0xffffff00, v104\n\tv_and_b32 v106, 0xffffff00, v106\n\t"
      "v_and_b32 v108, 0xffffff00, v108\n\tv_and_b32 v110, 0xffffff00, v110\n\tv_and_b32 v112, 0xffffff00, v112\n\tv_and_b32 v114, 0xffffff00, v114\n\t"
      "v_add_f32 v100, v100, v104\n\tv_add_f32 v102, v102, v106\n\tv_add_f32 v108, v108, v112\n\tv_add_f32 v110, v110, v114\n\t"
      "v_add_f32 %[r0], v100, v108\n\tv_add_f32 %[r1], v102, v110\n\t"
      "s_andn2_b64 %[pe], %[pe], vcc\n\t"
      "s_cbranch_scc0 2f\n\t"
      "s_mov_b64 exec, %[pe]\n\t"
      "s_sub_u32 %[cn], %[cn], 1\n\ts_cbranch_scc0 1b\n\t"
      "2:\n\ts_mov_b64 exec, %[sv]\n\t"
      : [r0] "+v"(r0), [r1] "+v"(r1), [t0] "=&v"(t0), [t1] "=&v"(t1), [t2] "=&v"(t2), [sv] "=&s"(sv), [pe] "=&s"(pend), [cn] "=&s"(cnt)
      : [vo] "v"(voff), [v1] "v"(voff + 4096u), [v2] "v"(voff + 8192u), [v3] "v"(voff + 12288u), [sb] "s"(sbase), [ta] "v"(taga), [tb] "v"(tagb)
      : "memory", "vcc", "scc", "v100", "v101", "v102", "v103", "v104", "v105", "v106", "v107", "v108", "v109", "v110", "v111", "v112", "v113", "v114", "v115");
}

#define STAMP(i) do {} while (0)
#define STAMP_DECL() do {} while (0)
#define STAMP_FLUSH() do {} while (0)

__device__ __forceinline__ float gelu_tanh(float x) {
  const float u = 0.7978845608028654f * (x + 0.044715f * x * x * x);
  const float e = ex2(u * (2.0f * LOG2E));
  const float t = 1.0f - 2.0f * __builtin_amdgcn_rcpf(e + 1.0f);
  return 0.5f * x * (1.0f + t);
}

__global__ void __launch_bounds__(NTHR) __attribute__((amdgpu_num_vgpr(100)))
fused_kernel(const u16* __restrict__ x, const u16* __restrict__ kc, const u16* __restrict__ vc, const u16* __restrict__ wq,
             const u16* __restrict__ wo, const u16* __restrict__ w1, const u16* __restrict__ b1, const u16* __restrict__ w2,
             const u16* __restrict__ b2, u16* __restrict__ out, unsigned char* __restrict__ ws) {
  extern __shared__ __attribute__((aligned(16))) unsigned char smem[];
  LAS unsigned char* L = (LAS unsigned char*)smem;
  const unsigned lbase = (unsigned)(size_t)L;
  volatile LAS unsigned* flags = (volatile LAS unsigned*)(L + L_FLAG);
  const int tid = threadIdx.x, lane = tid & 63, wv = __builtin_amdgcn_readfirstlane(tid >> 6);
  const int g = blockIdx.x & 7, j = blockIdx.x >> 3;
  const int b = j >> 4, sp = j & 15, rb = j >> 1, ch = j & 1;
  const int lg = lane >> 4, lr = lane & 15;
  const int me = g * 32 + j;
  const int rch = me >> 7, ru = (me & 127) * 2 + (lane & 1);
  const int rcol = rch * 256 + (ru >> 5) * 32 + (ru & 15), rbt = (ru >> 4) & 1;
  const bool hasw1 = (j >> 2) != 3;
  const int p1 = hasw1 ? (j < 12 ? j : j - 4) * 8 + g : 0;
  const size_t kvoff = (size_t)(b * HKV + g) * SK * (DH * 2);
  auto tile = [&](int idx) { return (unsigned)((((idx / TG) * 16 + sp) * TG + idx % TG + TSKEW * (b * HKV + g)) & 511) * 4096u; };
  const __amdgpu_buffer_rsrc_t rs = __builtin_amdgcn_make_buffer_rsrc((void*)ws, 0, (int)WS_END, 0x00020000);
  asm volatile("" ::"s"(x), "s"(kc), "s"(vc), "s"(wq), "s"(wo), "s"(w1), "s"(b1), "s"(w2), "s"(b2), "s"(out), "s"(ws));
  STAMP_DECL();
  STAMP(0);

  if (__builtin_expect(wv >= NAUX + NSTR, 1)) {
    const int ld = wv - (NAUX + NSTR);
    volatile LAS unsigned* prog = flags + F_VP + ld;
    __builtin_amdgcn_s_setprio(3);
    dma_w<4>((const unsigned char*)(wq + (size_t)((j & 1) * 256 + ld * 64) * (HQ * DH) + g * 512 + (j >> 1) * 32), (lane >> 2) * (HQ * DH * 2) + (lane & 3) * 16,
             16 * HQ * DH * 2, lbase + L_WST + ld * 4096);
    if (hasw1)
      dma_w<2>((const unsigned char*)(w1 + (size_t)((p1 & 3) * 128 + ld * 32) * FF + (p1 >> 2) * 32), (lane >> 2) * (FF * 2) + (lane & 3) * 16, 16 * FF * 2,
               lbase + L_WST + 16384 + ld * 2048);
    __builtin_amdgcn_s_setprio(0);
    BARRIER();
    WAIT_VM(0);
    LDS_INC(F_WIN);
    LDS_SPIN(flags[F_KISS] < (unsigned)VGATE);
    _Pragma("nounroll") for (int i = 0; i < QWAIT && flags[F_QISS] < 2u; ++i) __builtin_amdgcn_s_sleep(1);
    CBAR();
    STAMP(1);
    const unsigned char* vbase = (const unsigned char*)vc + kvoff;
    const unsigned dst = lbase + L_V + ld * 4096;
    const unsigned ve = lg * 256 + ((((lr >> 1) ^ lg) << 1) | (lr & 1)) * 16, vo = ve ^ 128u;
    _Pragma("nounroll") for (int c = 0; c < 8; ++c) {
      dma4_kv(vbase + tile(4 * c + ld), ve, vo, ve, vo, dst + c * 16384);
      if (c == 5 || c == 7) { WAIT_VM(16); *prog = (unsigned)(c - 3); }
    }
    STAMP(2);
    if (WLATE >= 6) { WAIT_VM(8); *prog = 6u; }
    if (WLATE >= 7) { WAIT_VM(4); *prog = 7u; }
    if (WLATE >= 8) { WAIT_VM(0); *prog = 8u; }
    {
      LDS_SPIN(flags[F_Q] < 2u);
      const int hl = lane >> 5, slot = lane & 31, rb2 = rb < 14 ? rb : 0;
      const unsigned char* wob = (const unsigned char*)(wo + (size_t)(g * 512 + rb * 32 + ld * 8) * DM + ch * 256);
      const unsigned char* w2b = (const unsigned char*)(w2 + (size_t)(g * 224 + rb2 * 16 + ld * 4) * DM + ch * 256);
      const unsigned base = hl * 1024 + slot * 16;
      _Pragma("nounroll") for (int i = 0; i < 4; ++i)
        dma1_w(wob + i * 2048, base ^ ((((ld & 1) << 2) | ((i & 1) << 1) | hl) << 5), lbase + L_WST + ld * 4096 + i * 1024);
      _Pragma("nounroll") for (int i = 0; i < 2; ++i)
        dma1_w(w2b + i * 2048, base ^ (((4 * ld + 2 * i + hl) & 7) << 5), lbase + L_WST + 16384 + ld * 2048 + i * 1024);
    }
    STAMP(4);
    if (WLATE < 6) { WAIT_VM(14); *prog = 6u; }
    if (WLATE < 7) { WAIT_VM(10); *prog = 7u; }
    if (WLATE < 8) { WAIT_VM(6); *prog = 8u; }
    WAIT_VM(0);
    STAMP(5);
    STAMP_FLUSH();
    return;
  }

  if (wv < 2) {
    __builtin_amdgcn_s_setprio(3);
    const bool isq = (wv == 0);
    const int rb2 = rb < 14 ? rb : 0;
    u32x4 xr0, xr1;
    u32x2 ep;
    unsigned bv;
    {
      const unsigned xo = lane * 32, zo = 0u, bo = (unsigned)((g * 224 + rb2 * 16 + 2 * (lane & 7)) * 2);
      asm volatile("global_load_dwordx4 %0, %4, %7\n\tglobal_load_dwordx4 %1, %4, %7 offset:16\n\t"
                   "global_load_dwordx2 %2, %5, %8 sc1\n\tglobal_load_dword %3, %6, %9"
                   : "=&v"(xr0), "=&v"(xr1), "=&v"(ep), "=&v"(bv) : "v"(xo), "v"(zo), "v"(bo), "s"(x), "s"(ws), "s"(b1) : "memory");
    }
    const int k0 = isq ? (j & 1) * 256 : (p1 & 3) * 128;
    const unsigned stg = isq ? L_WST : L_WST + 16384;
    if (isq) { *(LAS unsigned*)(L + L_Z + lane * 4) = 0u; if (lane < 16) flags[lane] = 0u; }
    BARRIER();
    STAMP(1);
    WAIT_VM(0);
    asm volatile("; PIN x" : "+v"(xr0), "+v"(xr1), "+v"(ep), "+v"(bv)::"memory");
    {
      float f[16];
#pragma unroll
      for (int i = 0; i < 4; ++i) { f[2 * i] = bflo(xr0[i]); f[2 * i + 1] = bfhi(xr0[i]); f[8 + 2 * i] = bflo(xr1[i]); f[9 + 2 * i] = bfhi(xr1[i]); }
      float s = 0.f;
#pragma unroll
      for (int i = 0; i < 16; ++i) s += f[i];
      const float mean = x16_sum(row_sum(s)) * (1.0f / DM);
      float q = 0.f;
#pragma unroll
      for (int i = 0; i < 16; ++i) { const float d = f[i] - mean; q += d * d; }
      const float var = x16_sum(row_sum(q)) * (1.0f / DM);
      const float mb = rbf(mean);
      const float inv = __builtin_amdgcn_rcpf(rbf(__builtin_amdgcn_sqrtf(rbf(rbf(var) + 1e-5f))));
      u32x4 o0, o1;
#pragma unroll
      for (int i = 0; i < 4; ++i) {
        const unsigned d0 = pack2(f[2 * i] - mb, f[2 * i + 1] - mb), d1 = pack2(f[8 + 2 * i] - mb, f[9 + 2 * i] - mb);
        o0[i] = pack2(bflo(d0) * inv, bfhi(d0) * inv);
        o1[i] = pack2(bflo(d1) * inv, bfhi(d1) * inv);
      }
      *(LAS u32x4*)(L + L_XN + wv * 2048 + lane * 32) = o0;
      *(LAS u32x4*)(L + L_XN + wv * 2048 + lane * 32 + 16) = o1;
    }
    const unsigned taga = ep[0] + 1u, tagb = (ep[1] + (taga == 0u ? 1u : 0u)) & 0xffu;
    STAMP(2);
    f32x4 acc[2] = {f32x4{0.f, 0.f, 0.f, 0.f}, f32x4{0.f, 0.f, 0.f, 0.f}};
    {
      unsigned ta = stg + (8 * lg + (lr >> 2)) * 64 + 8 * (lr & 3);
      unsigned xa = L_XN + wv * 2048 + (lr & 1) * 1024 + k0 * 2 + lg * 16;
      LDS_SPIN(flags[F_WIN] < 4u);
      _Pragma("nounroll") for (int hb = 0; hb < 2; ++hb) {
#pragma unroll
        for (int s = 0; s < 4; ++s) {
          const u32x4 xf = *(LAS u32x4*)(L + xa + s * 64);
#pragma unroll
          for (int t = 0; t < 2; ++t) {
            const u32x2 lo = tr_read(L, ta + s * 2048 + t * 32), hi = tr_read(L, ta + s * 2048 + t * 32 + 256);
            acc[t] = mfma32(u32x4{lo[0], lo[1], hi[0], hi[1]}, xf, acc[t]);
          }
        }
        if (!isq) break;
        ta += 8192; xa += 256;
      }
    }
    STAMP(3);
    if (lr < 8 && (isq || hasw1)) {
      const f32x4 a = (lr & 4) ? acc[1] : acc[0];
      const float v0 = (lr & 2) ? a[2] : a[0], v1 = (lr & 2) ? a[3] : a[1];
      const u32x4 gv = {g_lo(v0, tagb), taga, g_lo(v1, tagb), taga};
      const unsigned pb = isq ? WS_Q + (g * 2 + (j & 1)) * 4096 + (j >> 1) * 256 : WS_HP + (p1 >> 2) * 2048 + (p1 & 3) * 256;
      const unsigned sb = isq ? 65536u : 128u, st = isq ? 128u : 1024u;
      __builtin_amdgcn_raw_buffer_store_b128(gv, rs, (int)(pb + (lr & 1) * sb + (lr >> 2) * st + lg * 32 + (lr & 2) * 8), 0, AUX_SC1);
    }
    STAMP(4);
    __builtin_amdgcn_s_sleep(QSLEEP);
    LDS_SPIN(flags[F_KISS] < (unsigned)QGATE);
    u32x4 h0 = {0u, 0u, 0u, 0u};
    {
      const int qo = (int)(WS_Q + (b * 8 + g) * 8192) + wv * 2048 + lane * 16;
      const int ho = (int)(WS_HP + (g * 14 + rb2) * 1024) + lane * 16;
      u32x4 qv[4];
      bool ok[5];
#pragma unroll
      for (int i = 0; i < 4; ++i) { qv[i] = u32x4{0u, 0u, 0u, 0u}; ok[i] = false; }
      ok[4] = isq;
      _Pragma("nounroll") for (unsigned spins = 0; spins < AUX_CAP; ++spins) {
        CBAR();
#pragma unroll
        for (int i = 0; i < 4; ++i) if (!ok[i]) qv[i] = __builtin_amdgcn_raw_buffer_load_b128(rs, qo + (i & 1) * 1024 + (i >> 1) * 4096, 0, AUX_SC1);
        if (!ok[4]) h0 = __builtin_amdgcn_raw_buffer_load_b128(rs, ho, 0, AUX_SC1);
        if (spins == 0) LDS_INC(F_QISS);
        bool all = true;
#pragma unroll
        for (int i = 0; i < 4; ++i) { ok[i] = g_bad(qv[i], taga, tagb) == 0u; all = all && ok[i]; }
        ok[4] = isq || g_bad(h0, taga, tagb) == 0u;
        if (__all(all && ok[4])) break;
      }
#pragma unroll
      for (int i = 0; i < 2; ++i)
        *(LAS unsigned*)(L + L_Q + wv * 512 + i * 256 + lane * 4) = pack2(g_val(qv[i][0]) + g_val(qv[2 + i][0]), g_val(qv[i][2]) + g_val(qv[2 + i][2]));
      if (isq && lane == 0) { flags[F_TAGA] = taga; flags[F_TAGB] = tagb; flags[F_EP0] = ep[0]; flags[F_EP1] = ep[1]; }
      LDS_INC(F_Q);
    }
    STAMP(5);
    if (!isq) {
      if (lane < 2) {
        const float xb0 = bf2f(x[rbt * DM + rcol]) + bf2f(b2[rcol]), xb1 = bf2f(x[rbt * DM + rcol + 16]) + bf2f(b2[rcol + 16]);
        *(LAS f32x2*)(L + L_XB + lane * 8) = f32x2{xb0, xb1};
      }
      const float a0 = xrow_sum(g_val(h0[0])), a1 = xrow_sum(g_val(h0[2]));
      const unsigned hv = rb < 14 ? pack2(gelu_tanh(rbf(rbf(a0) + bflo(bv))), gelu_tanh(rbf(rbf(a1) + bfhi(bv)))) : 0u;
      if (lane < 16) *(LAS unsigned*)(L + L_H + lane * 4) = hv;
      STAMP(6);
      STAMP_FLUSH();
      return;
    }
    LDS_SPIN(VPROG() < (unsigned)MLTRIG);
    STAMP(6);
    {
      float ms = 0.f, ls = 0.f;
      const unsigned mo = WS_ML + (g * 32 + (lane & 31)) * 128 + (rb >> 2) * 16;
      const u64 miss = poll_pair<1, 0>(ws, mo, taga, tagb, ms, ls, 0xffffffffull, (unsigned)H1LEFT);
      flags[F_GO1] = 1u;
      STAMP(7);
      if (miss) poll_pair<1, 0>(ws, mo, taga, tagb, ms, ls, miss, 0u);
      const float e = ex2((ms - row_max(ms)) * LOG2E);
      const float w = e * __builtin_amdgcn_rcpf(row_sum(e * ls));
      if (lane < 32) *(LAS float*)(L + L_W + lane * 4) = w;
      STAMP(8);
      BARRIER();
      poll_pair<1, 0>(ws, WS_H2 + (rch * 128 + (lane & 7) * 16 + (j & 15)) * 4096 + ru * 16, taga, tagb, ms, ls, 0xffull, (unsigned)H2LEFT);
      flags[F_GO2] = 1u;
    }
    STAMP(9);
    STAMP_FLUSH();
    return;
  }

  const int sw = wv - NAUX;
  __builtin_amdgcn_s_setprio(2);
  if (KDELAY) __builtin_amdgcn_s_sleep(KDELAY);
  u32x4 kf[4][4];
  {
    const unsigned char* kbase = (const unsigned char*)kc + kvoff;
    const unsigned ko = lr * 256 + lg * 16;
#pragma unroll
    for (int t = 0; t < 4; ++t) {
      if (t == INITK) BARRIER();
      asm volatile("global_load_dwordx4 %0, %4, %5" NTS "\n\tglobal_load_dwordx4 %1, %4, %5 offset:64" NTS "\n\t"
                   "global_load_dwordx4 %2, %4, %5 offset:128" NTS "\n\tglobal_load_dwordx4 %3, %4, %5 offset:192" NTS
                   : "=&v"(kf[t][0]), "=&v"(kf[t][1]), "=&v"(kf[t][2]), "=&v"(kf[t][3]) : "v"(ko), "s"(kbase + tile(8 * t + sw)) : "memory");
    }
  }
  LDS_INC(F_KISS);
  STAMP(1);
  LDS_SPIN(flags[F_Q] < 2u);
  const unsigned taga = flags[F_TAGA], tagb = flags[F_TAGB];
  asm volatile("s_waitcnt vmcnt(0)"
               : "+v"(kf[0][0]), "+v"(kf[0][1]), "+v"(kf[0][2]), "+v"(kf[0][3]), "+v"(kf[1][0]), "+v"(kf[1][1]), "+v"(kf[1][2]), "+v"(kf[1][3]),
                 "+v"(kf[2][0]), "+v"(kf[2][1]), "+v"(kf[2][2]), "+v"(kf[2][3]), "+v"(kf[3][0]), "+v"(kf[3][1]), "+v"(kf[3][2]), "+v"(kf[3][3])
               :: "memory");
  STAMP(2);
  float mcu;
  {
    f32x4 a = {0.f, 0.f, 0.f, 0.f};
#pragma unroll
    for (int t = 0; t < 4; ++t) {
      const unsigned qa = (lr >> 2) == t ? L_Q + (lr & 3) * 256 + lg * 16 : L_Z;
#pragma unroll
      for (int s = 0; s < 4; ++s) a = mfma32(*(LAS u32x4*)(L + qa + s * 64), kf[t][s], a);
    }
    const unsigned r01 = pack2(a[0], a[1]), r23 = pack2(a[2], a[3]);
    const unsigned s01 = pack2(bflo(r01) * SCALE, bfhi(r01) * SCALE), s23 = pack2(bflo(r23) * SCALE, bfhi(r23) * SCALE);
    float sc[4] = {bflo(s01), bfhi(s01), bflo(s23), bfhi(s23)};
    const int hd = ((lg & 1) << 1) | (lg >> 1);
    {
      auto x = __builtin_amdgcn_permlane32_swap(__float_as_uint(sc[0]), __float_as_uint(sc[1]), false, false);
      auto y = __builtin_amdgcn_permlane32_swap(__float_as_uint(sc[2]), __float_as_uint(sc[3]), false, false);
      auto z = __builtin_amdgcn_permlane16_swap(__float_as_uint(fmaxf(__uint_as_float(x[0]), __uint_as_float(x[1]))),
                                                __float_as_uint(fmaxf(__uint_as_float(y[0]), __uint_as_float(y[1]))), false, false);
      const float m = row_max(fmaxf(__uint_as_float(z[0]), __uint_as_float(z[1])));
      if (lr == 0) *(LAS float*)(L + L_M + hd * 32 + sw * 4) = m;
    }
    LDS_INC(F_MAX);
    LDS_SPIN_HOT(flags[F_MAX] < 8u);
    {
      const f32x4 m0 = *(LAS f32x4*)(L + L_M + (lr & 3) * 32), m1 = *(LAS f32x4*)(L + L_M + (lr & 3) * 32 + 16);
      mcu = fmaxf(fmaxf(fmaxf(m0[0], m0[1]), fmaxf(m0[2], m0[3])), fmaxf(fmaxf(m1[0], m1[1]), fmaxf(m1[2], m1[3])));
    }
    sc[0] = ex2((sc[0] - dpp<0x00>(mcu)) * LOG2E);
    sc[1] = ex2((sc[1] - dpp<0x55>(mcu)) * LOG2E);
    sc[2] = ex2((sc[2] - dpp<0xAA>(mcu)) * LOG2E);
    sc[3] = ex2((sc[3] - dpp<0xFF>(mcu)) * LOG2E);
    {
      LAS u16* pw = (LAS u16*)(L + L_P + (4 * lg + (sw >> 1)) * 256 + (lr >> 2) * 16 + (sw & 1) * 8 + (lr & 3) * 2);
      const unsigned p01 = pack2(sc[0], sc[1]), p23 = pack2(sc[2], sc[3]);
      pw[0] = (u16)p01; pw[32] = (u16)(p01 >> 16); pw[64] = (u16)p23; pw[96] = (u16)(p23 >> 16);
      auto x = __builtin_amdgcn_permlane32_swap(__float_as_uint(sc[0]), __float_as_uint(sc[1]), false, false);
      auto y = __builtin_amdgcn_permlane32_swap(__float_as_uint(sc[2]), __float_as_uint(sc[3]), false, false);
      auto z = __builtin_amdgcn_permlane16_swap(__float_as_uint(__uint_as_float(x[0]) + __uint_as_float(x[1])),
                                                __float_as_uint(__uint_as_float(y[0]) + __uint_as_float(y[1])), false, false);
      const float l = row_sum(__uint_as_float(z[0]) + __uint_as_float(z[1]));
      if (lr == 0) *(LAS float*)(L + L_L + hd * 32 + sw * 4) = l;
    }
    LDS_INC(F_P);
  }
  STAMP(3);
  LDS_SPIN_HOT(flags[F_P] < 8u);
  u32x4 gml;
  {
    const f32x4 l0 = *(LAS f32x4*)(L + L_L + (lr & 3) * 32), l1 = *(LAS f32x4*)(L + L_L + (lr & 3) * 32 + 16);
    gml = u32x4{g_lo(mcu, tagb), taga, g_lo(((l0[0] + l0[1]) + (l0[2] + l0[3])) + ((l1[0] + l1[1]) + (l1[2] + l1[3])), tagb), taga};
    asm volatile("" : "+v"(gml));
  }
  f32x4 o = {0.f, 0.f, 0.f, 0.f};
  {
    unsigned vp = 0u;
    u32x4 pf[16];
#pragma unroll
    for (int s = 0; s < 16; ++s) pf[s] = *(LAS u32x4*)(L + L_P + s * 256 + (lr & 3) * 64 + lg * 16);
    const unsigned tb = L_V + (4 * lg + (lr >> 2)) * 256 + ((sw ^ (4 * (lg & 1) + (lr >> 2))) << 5) + 8 * (lr & 3);
#pragma unroll
    for (int s = 0; s < 16; ++s) {
      if (s == MLPOS && sw == 0 && lane < 4) __builtin_amdgcn_raw_buffer_store_b128(gml, rs, (int)(WS_ML + me * 128) + lane * 16, 0, AUX_SC1);
      if (s == 0) { VWAIT(2u); STAMP(4); }
      if (s == 4) VWAIT(4u);
      if (s == 8) VWAIT(6u);
      if (s == 12) VWAIT(7u);
      if (s == 14) { VWAIT(8u); STAMP(5); }
      const u32x2 lo = tr_read(L, tb + s * 8192), hi = tr_read(L, tb + s * 8192 + 4096);
      o = mfma32(u32x4{lo[0], lo[1], hi[0], hi[1]}, pf[s], o);
      if (s == 3 || s == 7 || s == 11 || s == 13) SCHED();
    }
  }
  if (lr < 8) {
    const float v0 = (lr & 4) ? o[2] : o[0], v1 = (lr & 4) ? o[3] : o[1];
    const u32x4 gv = {g_lo(v0, tagb), taga, g_lo(v1, tagb), taga};
    __builtin_amdgcn_raw_buffer_store_b128(gv, rs, (int)(WS_H1 + me * 4096 + sw * 128) + (lr & 3) * 1024 + lg * 32 + (lr & 4) * 4, 0, AUX_SC1);
  }
  if (MLPOS == 16 && sw == 0 && lane < 4) __builtin_amdgcn_raw_buffer_store_b128(gml, rs, (int)(WS_ML + me * 128) + lane * 16, 0, AUX_SC1);
  STAMP(7);
  {
    const unsigned oo = WS_H1 + ((g * 32 + sw * 4 + lg) * 4 + (rb >> 2)) * 1024 + (rb & 3) * 256 + lr * 16;
    float v0 = 0.f, v1 = 0.f;
    LDS_SPIN_HOT(flags[F_GO1] == 0u);
    poll_pair<PDEPTH, PSLEEP>(ws, oo, taga, tagb, v0, v1, ~0ull, 0u);
    STAMP(8);
    *(LAS unsigned*)(L + L_OX + (sw * 4 + lg) * 64 + lr * 4) = pack2(v0, v1);
  }
  BARRIER();
  {
    const int q = lr >> 2;
    const unsigned ta = L_WST + (8 * lg + q) * 512 + (((2 * sw) ^ (q | ((lg & 1) << 2))) << 5) + 8 * (lr & 3);
    const unsigned t2 = L_WST + 16384 + (4 * lg + q) * 512 + (((2 * sw) ^ ((4 * lg + q) & 7)) << 5) + 8 * (lr & 3);
    const u32x4 of0 = *(LAS u32x4*)(L + L_OX + lr * 64 + lg * 16), of1 = *(LAS u32x4*)(L + L_OX + 1024 + lr * 64 + lg * 16);
    const f32x4 w0 = *(LAS f32x4*)(L + L_W + lg * 16), w1v = *(LAS f32x4*)(L + L_W + 64 + lg * 16);
    const u32x2 hf = *(LAS u32x2*)(L + L_H + (lr & 1) * 32 + lg * 8);
    float r[2][2];
#pragma unroll
    for (int t = 0; t < 2; ++t) {
      const u32x2 lo = tr_read(L, ta ^ (t << 5)), hi = tr_read(L, (ta ^ (t << 5)) + 2048);
      const u32x2 w2f = tr_read(L, t2 ^ (t << 5));
      const u32x4 wf = {lo[0], lo[1], hi[0], hi[1]};
      const f32x4 a0 = mfma32(of0, wf, f32x4{0.f, 0.f, 0.f, 0.f}), a1 = mfma32(of1, wf, f32x4{0.f, 0.f, 0.f, 0.f});
      const f32x4 mac = mfma16(hf, w2f, f32x4{0.f, 0.f, 0.f, 0.f});
      const float p0 = (a0[0] * w0[0] + a0[1] * w0[1]) + (a0[2] * w0[2] + a0[3] * w0[3]);
      const float p1v = (a1[0] * w1v[0] + a1[1] * w1v[1]) + (a1[2] * w1v[2] + a1[3] * w1v[3]);
      auto sx = __builtin_amdgcn_permlane16_swap(__float_as_uint(p0), __float_as_uint(p1v), false, false);
      r[t][0] = x32_sum(__uint_as_float(sx[0]) + __uint_as_float(sx[1]));
      r[t][1] = (lg & 1) ? mac[1] : mac[0];
    }
    if (lane < 32) {
      const u32x4 gv = {g_lo(r[0][0] + r[0][1], tagb), taga, g_lo(r[1][0] + r[1][1], tagb), taga};
      __builtin_amdgcn_raw_buffer_store_b128(gv, rs, (int)(WS_H2 + ((ch * 128 + g * 16 + rb) * 8 + sw) * 512) + lane * 16, 0, AUX_SC1);
    }
  }
  STAMP(9);
  if (sw == 0) {
    const unsigned ho = WS_H2 + (rch * 128 + (lane >> 1) * 4) * 4096 + ru * 16;
    const f32x2 xb = *(LAS f32x2*)(L + L_XB + (lane & 1) * 8);
    float s0 = 0.f, s1 = 0.f;
    LDS_SPIN_HOT(flags[F_GO2] == 0u);
    poll_quad(ws, ho, taga, tagb, s0, s1);
    STAMP(10);
    s0 += dpp<XOR2>(s0); s1 += dpp<XOR2>(s1);
    s0 += dpp<ROR4>(s0); s1 += dpp<ROR4>(s1);
    s0 += dpp<ROR8>(s0); s1 += dpp<ROR8>(s1);
    s0 = xrow_sum(s0); s1 = xrow_sum(s1);
    if (lane < 2) {
      out[rbt * DM + rcol] = (u16)f2bf(xb[0] + s0);
      out[rbt * DM + rcol + 16] = (u16)f2bf(xb[1] + s1);
    }
    if (me == 0 && lane == 0) {
      const u64 e = (((u64)flags[F_EP1] << 32) | flags[F_EP0]) + 1ull;
      __hip_atomic_store((GAS u64*)(ws + WS_EPOCH), e, __ATOMIC_RELAXED, __HIP_MEMORY_SCOPE_AGENT);
    }
    STAMP(11);
  }
  STAMP_FLUSH();
}

extern "C" void kernel_launch(void* const* d_in, const int* in_sizes, int n_in, void* d_out, int out_size, void* d_ws, size_t ws_size,
                              hipStream_t stream) {
  (void)hipFuncSetAttribute((const void*)fused_kernel, hipFuncAttributeMaxDynamicSharedMemorySize, L_BYTES);
  fused_kernel<<<256, NTHR, L_BYTES, stream>>>((const u16*)d_in[0], (const u16*)d_in[1], (const u16*)d_in[2], (const u16*)d_in[3],
                                               (const u16*)d_in[4], (const u16*)d_in[6], (const u16*)d_in[7], (const u16*)d_in[8],
                                               (const u16*)d_in[9], (u16*)d_out, (unsigned char*)d_ws);
}
```

```cpp
#include <hip/hip_runtime.h>
#include <hip/hip_bf16.h>
#include <stdint.h>

#define GAS __attribute__((address_space(1)))
#define LAS __attribute__((address_space(3)))
typedef unsigned u32x4 __attribute__((ext_vector_type(4)));
typedef unsigned u32x2 __attribute__((ext_vector_type(2)));
typedef float f32x4 __attribute__((ext_vector_type(4)));
typedef float f32x2 __attribute__((ext_vector_type(2)));
typedef short s16x4 __attribute__((ext_vector_type(4)));
typedef __bf16 bf16x8 __attribute__((ext_vector_type(8)));
typedef unsigned short u16;
typedef unsigned long long u64;

constexpr int DM = 512, HQ = 32, HKV = 8, SK = 8192, DH = 128, FF = 1792;
constexpr int NAUX = 2, NSTR = 8, NLD = 4, NTHR = (NAUX + NSTR + NLD) * 64;
constexpr float SCALE = 0.08838834764831845f, LOG2E = 1.4426950408889634f;
constexpr unsigned SPIN_CAP = 1u << 17;
constexpr int TG = 8, TSKEW = 32;
constexpr int TV0 = 490, DV = 62, TW = 990;
constexpr int QSLEEP = 24, QWAIT = 12;
constexpr unsigned H1LEFT = 3, H2LEFT = 1;

constexpr unsigned WS_EPOCH = 0;
constexpr unsigned WS_ML = 393216;
constexpr unsigned WS_Q = 32768;
constexpr unsigned WS_HP = WS_Q + 131072;
constexpr unsigned WS_H1 = 524288;
constexpr unsigned WS_H2 = WS_H1 + (1u << 20);
constexpr unsigned WS_END = WS_H2 + (1u << 20);

constexpr unsigned L_V = 0;
constexpr unsigned L_WST = 131072;
constexpr unsigned L_XN = L_WST + 24576;
constexpr unsigned L_P = L_XN;
constexpr unsigned L_Q = L_XN + 4096;
constexpr unsigned L_OX = L_Q + 1024;
constexpr unsigned L_M = L_OX + 2048;
constexpr unsigned L_L = L_M + 128;
constexpr unsigned L_W = L_L + 128;
constexpr unsigned L_H = L_W + 128;
constexpr unsigned L_XB = L_H + 64;
constexpr unsigned L_FLAG = L_XB + 32;
constexpr unsigned L_BYTES = L_FLAG + 64;
static_assert(L_BYTES <= 160 * 1024, "LDS");
enum { F_VP = 0, F_Q = 4, F_MAX = 5, F_P = 6, F_KISS = 7, F_QISS = 12, F_GO1 = 14, F_GO2 = 15, F_WIN = 13, F_TAGA = 8, F_TAGB = 9, F_EP0 = 10, F_EP1 = 11 };

__device__ __forceinline__ float bf2f(unsigned b) { return __builtin_bit_cast(float, b << 16); }
__device__ __forceinline__ float bflo(unsigned u) { return __builtin_bit_cast(float, u << 16); }
__device__ __forceinline__ float bfhi(unsigned u) { return __builtin_bit_cast(float, u & 0xffff0000u); }
__device__ __forceinline__ unsigned f2bf(float f) { return (unsigned)__builtin_bit_cast(u16, (__bf16)f); }
__device__ __forceinline__ float rbf(float f) { return bf2f(f2bf(f)); }
__device__ __forceinline__ unsigned pack2(float lo, float hi) {
  typedef __bf16 bf16x2 __attribute__((ext_vector_type(2)));
  const f32x2 v = {lo, hi};
  return __builtin_bit_cast(unsigned, __builtin_convertvector(v, bf16x2));
}
__device__ __forceinline__ f32x4 mfma32(u32x4 a, u32x4 b, f32x4 c) {
  return __builtin_amdgcn_mfma_f32_16x16x32_bf16(__builtin_bit_cast(bf16x8, a), __builtin_bit_cast(bf16x8, b), c, 0, 0, 0);
}
__device__ __forceinline__ f32x4 mfma16(u32x2 a, u32x2 b, f32x4 c) {
  return __builtin_amdgcn_mfma_f32_16x16x16bf16_1k(__builtin_bit_cast(s16x4, a), __builtin_bit_cast(s16x4, b), c, 0, 0, 0);
}
template <int CTRL> __device__ __forceinline__ float dpp(float x) {
  return __builtin_bit_cast(float, __builtin_amdgcn_mov_dpp(__builtin_bit_cast(int, x), CTRL, 0xf, 0xf, true));
}
constexpr int XOR1 = 0xB1, XOR2 = 0x4E, HMIR = 0x141, MIR = 0x140, ROR4 = 0x124, ROR8 = 0x128;
__device__ __forceinline__ float row_sum(float v) { v += dpp<XOR1>(v); v += dpp<XOR2>(v); v += dpp<HMIR>(v); v += dpp<MIR>(v); return v; }
__device__ __forceinline__ float row_max(float v) {
  v = fmaxf(v, dpp<XOR1>(v)); v = fmaxf(v, dpp<XOR2>(v)); v = fmaxf(v, dpp<HMIR>(v)); v = fmaxf(v, dpp<MIR>(v)); return v;
}
__device__ __forceinline__ float x16_sum(float x) {
  auto s = __builtin_amdgcn_permlane16_swap(__float_as_uint(x), __float_as_uint(x), false, false);
  return __uint_as_float(s[0]) + __uint_as_float(s[1]);
}
__device__ __forceinline__ float x32_sum(float x) {
  auto t = __builtin_amdgcn_permlane32_swap(__float_as_uint(x), __float_as_uint(x), false, false);
  return __uint_as_float(t[0]) + __uint_as_float(t[1]);
}
__device__ __forceinline__ float xrow_sum(float x) { return x32_sum(x16_sum(x)); }
__device__ __forceinline__ float xrow_max(float x) {
  auto s = __builtin_amdgcn_permlane16_swap(__float_as_uint(x), __float_as_uint(x), false, false);
  x = fmaxf(__uint_as_float(s[0]), __uint_as_float(s[1]));
  auto t = __builtin_amdgcn_permlane32_swap(__float_as_uint(x), __float_as_uint(x), false, false);
  return fmaxf(__uint_as_float(t[0]), __uint_as_float(t[1]));
}
__device__ __forceinline__ float ex2(float x) { return __builtin_amdgcn_exp2f(x); }

#define CBAR() asm volatile("" ::: "memory")
#define SCHED() __builtin_amdgcn_sched_barrier(0)
#define WAIT_VM(n) asm volatile("s_waitcnt vmcnt(" #n ")" ::: "memory")
#define BARRIER() do { asm volatile("s_waitcnt lgkmcnt(0)" ::: "memory"); __builtin_amdgcn_s_barrier(); CBAR(); } while (0)
#define LDS_SPIN(cond) do { _Pragma("nounroll") for (unsigned spins_ = 0; spins_ < (SPIN_CAP << 4) && (cond); ++spins_) __builtin_amdgcn_s_sleep(1); CBAR(); } while (0)
#define LDS_SPIN_HOT(cond) do { _Pragma("nounroll") for (unsigned spins_ = 0; spins_ < (SPIN_CAP << 5) && (cond); ++spins_) {} CBAR(); } while (0)
#define VPROG() vprog(*(volatile LAS u32x4*)(L + L_FLAG))
#define VWAIT(n) do { _Pragma("nounroll") for (unsigned spins_ = 0; spins_ < (SPIN_CAP << 5) && vp < (n); ++spins_) vp = __builtin_amdgcn_readfirstlane(VPROG()); CBAR(); } while (0)
#define LDS_INC(i) do { CBAR(); if (lane == 0) __hip_atomic_fetch_add((LAS unsigned*)(L + L_FLAG + 4 * (i)), 1u, __ATOMIC_RELAXED, __HIP_MEMORY_SCOPE_WORKGROUP); CBAR(); } while (0)

__device__ __forceinline__ void dma4_kv(const void* sbase, unsigned v0, unsigned v1, unsigned v2, unsigned v3, unsigned dst) {
  unsigned keep;
  asm volatile(
      "s_mov_b32 %0, m0\n\ts_mov_b32 m0, %6\n\ts_nop 0\n\t"
      "global_load_lds_dwordx4 %1, %5\n\t"
      "global_load_lds_dwordx4 %2, %5 offset:1024\n\t"
      "global_load_lds_dwordx4 %3, %5 offset:2048\n\t"
      "global_load_lds_dwordx4 %4, %5 offset:3072\n\t"
      "s_mov_b32 m0, %0"
      : "=&s"(keep) : "v"(v0), "v"(v1), "v"(v2), "v"(v3), "s"(sbase), "s"(dst) : "memory");
}
#define DMA_STEP "global_load_lds_dwordx4 %1, %2\n\ts_add_u32 m0, m0, 0x400\n\tv_add_u32 %1, %4, %1\n\t"
template <int N> __device__ __forceinline__ void dma_w(const void* sbase, unsigned voff, unsigned step, unsigned dst) {
  unsigned keep;
  if constexpr (N == 4)
    asm volatile("s_mov_b32 %0, m0\n\ts_mov_b32 m0, %3\n\ts_nop 0\n\t" DMA_STEP DMA_STEP DMA_STEP DMA_STEP "s_mov_b32 m0, %0"
                 : "=&s"(keep), "+v"(voff) : "s"(sbase), "s"(dst), "s"(step) : "memory", "scc");
  else
    asm volatile("s_mov_b32 %0, m0\n\ts_mov_b32 m0, %3\n\ts_nop 0\n\t" DMA_STEP DMA_STEP "s_mov_b32 m0, %0"
                 : "=&s"(keep), "+v"(voff) : "s"(sbase), "s"(dst), "s"(step) : "memory", "scc");
}
__device__ __forceinline__ void dma1_w(const void* sbase, unsigned voff, unsigned dst) {
  unsigned keep;
  asm volatile("s_mov_b32 %0, m0\n\ts_mov_b32 m0, %3\n\ts_nop 0\n\tglobal_load_lds_dwordx4 %1, %2\n\ts_mov_b32 m0, %0"
               : "=&s"(keep) : "v"(voff), "s"(sbase), "s"(dst) : "memory");
}
__device__ __forceinline__ u32x2 tr_read(LAS unsigned char* L, unsigned a) {
  return __builtin_bit_cast(u32x2, __builtin_amdgcn_ds_read_tr16_b64_v4i16((LAS s16x4*)(L + a)));
}
__device__ __forceinline__ unsigned g_lo(float v, unsigned tagb) {
  unsigned u = __builtin_bit_cast(unsigned, v);
  u += 0x7fu + ((u >> 8) & 1u);
  return (u & 0xffffff00u) | tagb;
}
__device__ __forceinline__ float g_val(unsigned lo) { return __builtin_bit_cast(float, lo & 0xffffff00u); }
__device__ __forceinline__ unsigned g_bad(u32x4 v, unsigned taga, unsigned tagb) {
  return (v[1] ^ taga) | (v[3] ^ taga) | (((v[0] ^ tagb) | (v[2] ^ tagb)) & 0xffu);
}
__device__ __forceinline__ unsigned vprog(u32x4 v) { return min(min(v[0], v[1]), min(v[2], v[3])); }
constexpr int AUX_SC1 = 16;

__device__ __forceinline__ u64 poll_pair(const void* sbase, unsigned voff, unsigned taga, unsigned tagb, float& r0, float& r1, u64 pend, unsigned left) {
  unsigned t0, t1, t2, cnt, nb;
  u64 sv;
  asm volatile(
      "s_mov_b64 %[sv], exec\n\ts_mov_b32 %[cn], 0x8000\n\t"
      "1:\n\t"
      "s_mov_b64 exec, %[pe]\n\t"
      "global_load_dwordx4 v[100:103], %[vo], %[sb] sc1\n\t"
      "s_waitcnt vmcnt(0)\n\t"
      "v_xor_b32 %[t0], v101, %[ta]\n\tv_xor_b32 %[t1], v100, %[tb]\n\tv_xor_b32 %[t2], v102, %[tb]\n\tv_or_b32 %[t1], %[t1], %[t2]\n\t"
      "v_xor_b32 %[t2], v103, %[ta]\n\tv_and_b32 %[t1], 0xff, %[t1]\n\tv_or3_b32 %[t0], %[t0], %[t1], %[t2]\n\t"
      "v_cmp_eq_u32 vcc, 0, %[t0]\n\t"
      "s_mov_b64 exec, vcc\n\t"
      "v_and_b32 %[r0], 0xffffff00, v100\n\tv_and_b32 %[r1], 0xffffff00, v102\n\t"
      "s_andn2_b64 %[pe], %[pe], vcc\n\t"
      "s_bcnt1_i32_b64 %[nb], %[pe]\n\t"
      "s_cmp_le_u32 %[nb], %[lf]\n\t"
      "s_cbranch_scc1 2f\n\t"
      "s_sub_u32 %[cn], %[cn], 1\n\ts_cbranch_scc0 1b\n\t"
      "2:\n\ts_mov_b64 exec, %[sv]\n\t"
      : [r0] "+v"(r0), [r1] "+v"(r1), [t0] "=&v"(t0), [t1] "=&v"(t1), [t2] "=&v"(t2), [sv] "=&s"(sv), [pe] "+s"(pend), [cn] "=&s"(cnt), [nb] "=&s"(nb)
      : [vo] "v"(voff), [sb] "s"(sbase), [ta] "v"(taga), [tb] "v"(tagb), [lf] "s"(left)
      : "memory", "vcc", "scc", "v100", "v101", "v102", "v103");
  return pend;
}

#define QLOAD "global_load_dwordx4 v[100:103], %[vo], %[sb] sc1\n\tglobal_load_dwordx4 v[104:107], %[v1], %[sb] sc1\n\t" \
              "global_load_dwordx4 v[108:111], %[v2], %[sb] sc1\n\tglobal_load_dwordx4 v[112:115], %[v3], %[sb] sc1\n\t"
__device__ __forceinline__ void poll_quad(const void* sbase, unsigned voff, unsigned taga, unsigned tagb, float& r0, float& r1) {
  unsigned t0, t1, t2;
  u64 sv, pend;
  unsigned cnt;
  asm volatile(
      "s_mov_b64 %[sv], exec\n\ts_mov_b64 %[pe], exec\n\ts_mov_b32 %[cn], 0x8000\n\t"
      "1:\n\t" QLOAD
      "s_waitcnt vmcnt(0)\n\t"
      "v_xor_b32 %[t0], v101, %[ta]\n\tv_xor_b32 %[t1], v100, %[tb]\n\t"
      "v_bitop3_b32 %[t0], v103, %[ta], %[t0] bitop3:0xbe\n\tv_bitop3_b32 %[t1], v102, %[tb], %[t1] bitop3:0xbe\n\t"
      "v_bitop3_b32 %[t0], v105, %[ta], %[t0] bitop3:0xbe\n\tv_bitop3_b32 %[t1], v104, %[tb], %[t1] bitop3:0xbe\n\t"
      "v_bitop3_b32 %[t0], v107, %[ta], %[t0] bitop3:0xbe\n\tv_bitop3_b32 %[t1], v106, %[tb], %[t1] bitop3:0xbe\n\t"
      "v_bitop3_b32 %[t0], v109, %[ta], %[t0] bitop3:0xbe\n\tv_bitop3_b32 %[t1], v108, %[tb], %[t1] bitop3:0xbe\n\t"
      "v_bitop3_b32 %[t0], v111, %[ta], %[t0] bitop3:0xbe\n\tv_bitop3_b32 %[t1], v110, %[tb], %[t1] bitop3:0xbe\n\t"
      "v_bitop3_b32 %[t0], v113, %[ta], %[t0] bitop3:0xbe\n\tv_bitop3_b32 %[t1], v112, %[tb], %[t1] bitop3:0xbe\n\t"
      "v_bitop3_b32 %[t0], v115, %[ta], %[t0] bitop3:0xbe\n\tv_bitop3_b32 %[t1], v114, %[tb], %[t1] bitop3:0xbe\n\t"
      "v_and_b32 %[t1], 0xff, %[t1]\n\t"
      "v_or_b32 %[t0], %[t0], %[t1]\n\t"
      "v_cmp_eq_u32 vcc, 0, %[t0]\n\t"
      "s_mov_b64 exec, vcc\n\t"
      "v_and_b32 v100, 0xffffff00, v100\n\tv_and_b32 v102, 0xffffff00, v102\n\tv_and_b32 v104, 0xffffff00, v104\n\tv_and_b32 v106, 0xffffff00, v106\n\t"
      "v_and_b32 v108, 0xffffff00, v108\n\tv_and_b32 v110, 0xffffff00, v110\n\tv_and_b32 v112, 0xffffff00, v112\n\tv_and_b32 v114, 0xffffff00, v114\n\t"
      "v_add_f32 v100, v100, v104\n\tv_add_f32 v102, v102, v106\n\tv_add_f32 v108, v108, v112\n\tv_add_f32 v110, v110, v114\n\t"
      "v_add_f32 %[r0], v100, v108\n\tv_add_f32 %[r1], v102, v110\n\t"
      "s_andn2_b64 %[pe], %[pe], vcc\n\t"
      "s_cbranch_scc0 2f\n\t"
      "s_mov_b64 exec, %[pe]\n\t"
      "s_sub_u32 %[cn], %[cn], 1\n\ts_cbranch_scc0 1b\n\t"
      "2:\n\ts_mov_b64 exec, %[sv]\n\t"
      : [r0] "+v"(r0), [r1] "+v"(r1), [t0] "=&v"(t0), [t1] "=&v"(t1), [t2] "=&v"(t2), [sv] "=&s"(sv), [pe] "=&s"(pend), [cn] "=&s"(cnt)
      : [vo] "v"(voff), [v1] "v"(voff + 4096u), [v2] "v"(voff + 8192u), [v3] "v"(voff + 12288u), [sb] "s"(sbase), [ta] "v"(taga), [tb] "v"(tagb)
      : "memory", "vcc", "scc", "v100", "v101", "v102", "v103", "v104", "v105", "v106", "v107", "v108", "v109", "v110", "v111", "v112", "v113", "v114", "v115");
}


__device__ __forceinline__ float gelu_tanh(float x) {
  const float u = 0.7978845608028654f * (x + 0.044715f * x * x * x);
  const float e = ex2(u * (2.0f * LOG2E));
  const float t = 1.0f - 2.0f * __builtin_amdgcn_rcpf(e + 1.0f);
  return 0.5f * x * (1.0f + t);
}

__global__ void __launch_bounds__(NTHR) __attribute__((amdgpu_num_vgpr(100)))
fused_kernel(const u16* __restrict__ x, const u16* __restrict__ kc, const u16* __restrict__ vc, const u16* __restrict__ wq,
             const u16* __restrict__ wo, const u16* __restrict__ w1, const u16* __restrict__ b1, const u16* __restrict__ w2,
             const u16* __restrict__ b2, u16* __restrict__ out, unsigned char* __restrict__ ws) {
  extern __shared__ __attribute__((aligned(16))) unsigned char smem[];
  LAS unsigned char* L = (LAS unsigned char*)smem;
  const unsigned lbase = (unsigned)(size_t)L;
  volatile LAS unsigned* flags = (volatile LAS unsigned*)(L + L_FLAG);
  const int tid = threadIdx.x, lane = tid & 63, wv = __builtin_amdgcn_readfirstlane(tid >> 6);
  const int g = blockIdx.x & 7, j = blockIdx.x >> 3;
  const int b = j >> 4, sp = j & 15, rb = j >> 1, ch = j & 1;
  const int lg = lane >> 4, lr = lane & 15;
  const int me = g * 32 + j;
  const int rch = me >> 7, ru = (me & 127) * 2 + (lane & 1);
  const int rcol = rch * 256 + (ru >> 5) * 32 + (ru & 15), rbt = (ru >> 4) & 1;
  const bool hasw1 = (j >> 2) != 3;
  const int p1 = hasw1 ? (j < 12 ? j : j - 4) * 8 + g : 0;
  const size_t kvoff = (size_t)(b * HKV + g) * SK * (DH * 2);
  auto tile = [&](int idx) { return (unsigned)((((idx / TG) * 16 + ((sp + idx / TG) & 15)) * TG + idx % TG + TSKEW * (b * HKV + g)) & 511) * 4096u; };
  const __amdgpu_buffer_rsrc_t rs = __builtin_amdgcn_make_buffer_rsrc((void*)ws, 0, (int)WS_END, 0x00020000);
  asm volatile("" ::"s"(x), "s"(kc), "s"(vc), "s"(wq), "s"(wo), "s"(w1), "s"(b1), "s"(w2), "s"(b2), "s"(out), "s"(ws));

  if (wv >= NAUX + NSTR) {
    const int ld = wv - (NAUX + NSTR);
    volatile LAS unsigned* prog = flags + F_VP + ld;
    const u64 tstart = __builtin_amdgcn_s_memrealtime();
    __builtin_amdgcn_s_setprio(3);
    dma_w<4>((const unsigned char*)(wq + (size_t)((j & 1) * 256 + ld * 64) * (HQ * DH) + g * 512 + (j >> 1) * 32), (lane >> 2) * (HQ * DH * 2) + (lane & 3) * 16,
             16 * HQ * DH * 2, lbase + L_WST + ld * 4096);
    if (hasw1)
      dma_w<2>((const unsigned char*)(w1 + (size_t)((p1 & 3) * 128 + ld * 32) * FF + (p1 >> 2) * 32), (lane >> 2) * (FF * 2) + (lane & 3) * 16, 16 * FF * 2,
               lbase + L_WST + 16384 + ld * 2048);
    __builtin_amdgcn_s_setprio(0);
    BARRIER();
    WAIT_VM(0);
    LDS_INC(F_WIN);
    LDS_SPIN(flags[F_KISS] < 8u);
    _Pragma("nounroll") for (int i = 0; i < QWAIT && flags[F_QISS] < 2u; ++i) __builtin_amdgcn_s_sleep(1);
    CBAR();
    const unsigned char* vbase = (const unsigned char*)vc + kvoff;
    const unsigned dst = lbase + L_V + ld * 4096;
    const unsigned ve = lg * 256 + ((((lr >> 1) ^ lg) << 1) | (lr & 1)) * 16, vo = ve ^ 128u;
    _Pragma("nounroll") for (int c = 0; c < 8; ++c) {
      while (__builtin_amdgcn_s_memrealtime() - tstart < (u64)(TV0 + DV * c)) __builtin_amdgcn_s_sleep(1);
      dma4_kv(vbase + tile(4 * c + ld), ve, vo, ve, vo, dst + c * 16384);
      if (c == 5 || c == 7) { WAIT_VM(16); *prog = (unsigned)(c - 3); }
    }
    {
      LDS_SPIN(flags[F_Q] < 2u);
      while (__builtin_amdgcn_s_memrealtime() - tstart < (u64)TW) __builtin_amdgcn_s_sleep(1);
      const int hl = lane >> 5, slot = lane & 31, rb2 = rb < 14 ? rb : 0;
      const unsigned char* wob = (const unsigned char*)(wo + (size_t)(g * 512 + rb * 32 + ld * 8) * DM + ch * 256);
      const unsigned char* w2b = (const unsigned char*)(w2 + (size_t)(g * 224 + rb2 * 16 + ld * 4) * DM + ch * 256);
      const unsigned base = hl * 1024 + slot * 16;
      _Pragma("nounroll") for (int i = 0; i < 4; ++i)
        dma1_w(wob + i * 2048, base ^ ((((ld & 1) << 2) | ((i & 1) << 1) | hl) << 5), lbase + L_WST + ld * 4096 + i * 1024);
      _Pragma("nounroll") for (int i = 0; i < 2; ++i)
        dma1_w(w2b + i * 2048, base ^ (((4 * ld + 2 * i + hl) & 7) << 5), lbase + L_WST + 16384 + ld * 2048 + i * 1024);
    }
    WAIT_VM(14); *prog = 6u;
    WAIT_VM(10); *prog = 7u;
    WAIT_VM(6); *prog = 8u;
    WAIT_VM(0);
    return;
  }

  if (wv < 2) {
    __builtin_amdgcn_s_setprio(3);
    const bool isq = (wv == 0);
    const int rb2 = rb < 14 ? rb : 0;
    u32x4 xr0, xr1;
    u32x2 ep;
    unsigned bv;
    {
      const unsigned xo = lane * 32, zo = 0u, bo = (unsigned)((g * 224 + rb2 * 16 + 2 * (lane & 7)) * 2);
      asm volatile("global_load_dwordx4 %0, %4, %7\n\tglobal_load_dwordx4 %1, %4, %7 offset:16\n\t"
                   "global_load_dwordx2 %2, %5, %8 sc1\n\tglobal_load_dword %3, %6, %9"
                   : "=&v"(xr0), "=&v"(xr1), "=&v"(ep), "=&v"(bv) : "v"(xo), "v"(zo), "v"(bo), "s"(x), "s"(ws), "s"(b1) : "memory");
    }
    const int k0 = isq ? (j & 1) * 256 : (p1 & 3) * 128;
    const unsigned stg = isq ? L_WST : L_WST + 16384;
    if (isq && lane < 16) flags[lane] = 0u;
    BARRIER();
    WAIT_VM(0);
    asm volatile("; PIN x" : "+v"(xr0), "+v"(xr1), "+v"(ep), "+v"(bv)::"memory");
    {
      float f[16];
#pragma unroll
      for (int i = 0; i < 4; ++i) { f[2 * i] = bflo(xr0[i]); f[2 * i + 1] = bfhi(xr0[i]); f[8 + 2 * i] = bflo(xr1[i]); f[9 + 2 * i] = bfhi(xr1[i]); }
      float s = 0.f;
#pragma unroll
      for (int i = 0; i < 16; ++i) s += f[i];
      const float mean = x16_sum(row_sum(s)) * (1.0f / DM);
      float q = 0.f;
#pragma unroll
      for (int i = 0; i < 16; ++i) { const float d = f[i] - mean; q += d * d; }
      const float var = x16_sum(row_sum(q)) * (1.0f / DM);
      const float mb = rbf(mean);
      const float inv = __builtin_amdgcn_rcpf(rbf(__builtin_amdgcn_sqrtf(rbf(rbf(var) + 1e-5f))));
      u32x4 o0, o1;
#pragma unroll
      for (int i = 0; i < 4; ++i) {
        const unsigned d0 = pack2(f[2 * i] - mb, f[2 * i + 1] - mb), d1 = pack2(f[8 + 2 * i] - mb, f[9 + 2 * i] - mb);
        o0[i] = pack2(bflo(d0) * inv, bfhi(d0) * inv);
        o1[i] = pack2(bflo(d1) * inv, bfhi(d1) * inv);
      }
      *(LAS u32x4*)(L + L_XN + wv * 2048 + lane * 32) = o0;
      *(LAS u32x4*)(L + L_XN + wv * 2048 + lane * 32 + 16) = o1;
    }
    const unsigned taga = ep[0] + 1u, tagb = (ep[1] + (taga == 0u ? 1u : 0u)) & 0xffu;
    f32x4 acc[2] = {f32x4{0.f, 0.f, 0.f, 0.f}, f32x4{0.f, 0.f, 0.f, 0.f}};
    {
      unsigned ta = stg + (8 * lg + (lr >> 2)) * 64 + 8 * (lr & 3);
      unsigned xa = L_XN + wv * 2048 + (lr & 1) * 1024 + k0 * 2 + lg * 16;
      LDS_SPIN(flags[F_WIN] < 4u);
      _Pragma("nounroll") for (int hb = 0; hb < 2; ++hb) {
#pragma unroll
        for (int s = 0; s < 4; ++s) {
          const u32x4 xf = *(LAS u32x4*)(L + xa + s * 64);
#pragma unroll
          for (int t = 0; t < 2; ++t) {
            const u32x2 lo = tr_read(L, ta + s * 2048 + t * 32), hi = tr_read(L, ta + s * 2048 + t * 32 + 256);
            acc[t] = mfma32(u32x4{lo[0], lo[1], hi[0], hi[1]}, xf, acc[t]);
          }
        }
        if (!isq) break;
        ta += 8192; xa += 256;
      }
    }
    if (lr < 8 && (isq || hasw1)) {
      const f32x4 a = (lr & 4) ? acc[1] : acc[0];
      const float v0 = (lr & 2) ? a[2] : a[0], v1 = (lr & 2) ? a[3] : a[1];
      const u32x4 gv = {g_lo(v0, tagb), taga, g_lo(v1, tagb), taga};
      const unsigned pb = isq ? WS_Q + (g * 2 + (j & 1)) * 4096 + (j >> 1) * 256 : WS_HP + (p1 >> 2) * 2048 + (p1 & 3) * 256;
      const unsigned sb = isq ? 65536u : 128u, st = isq ? 128u : 1024u;
      __builtin_amdgcn_raw_buffer_store_b128(gv, rs, (int)(pb + (lr & 1) * sb + (lr >> 2) * st + lg * 32 + (lr & 2) * 8), 0, AUX_SC1);
    }
    __builtin_amdgcn_s_sleep(QSLEEP);
    LDS_SPIN(flags[F_KISS] < 8u);
    u32x4 h0 = {0u, 0u, 0u, 0u};
    {
      const int qo = (int)(WS_Q + (b * 8 + g) * 8192) + wv * 2048 + lane * 16;
      const int ho = (int)(WS_HP + (g * 14 + rb2) * 1024) + lane * 16;
      u32x4 qv[4];
      bool ok[5];
#pragma unroll
      for (int i = 0; i < 4; ++i) { qv[i] = u32x4{0u, 0u, 0u, 0u}; ok[i] = false; }
      const bool noh = isq || rb >= 14;
      ok[4] = noh;
      _Pragma("nounroll") for (unsigned spins = 0; spins < (SPIN_CAP >> 2); ++spins) {
        CBAR();
#pragma unroll
        for (int i = 0; i < 4; ++i) if (!ok[i]) qv[i] = __builtin_amdgcn_raw_buffer_load_b128(rs, qo + (i & 1) * 1024 + (i >> 1) * 4096, 0, AUX_SC1);
        if (!ok[4]) h0 = __builtin_amdgcn_raw_buffer_load_b128(rs, ho, 0, AUX_SC1);
        if (spins == 0) LDS_INC(F_QISS);
        bool all = true;
#pragma unroll
        for (int i = 0; i < 4; ++i) { ok[i] = g_bad(qv[i], taga, tagb) == 0u; all = all && ok[i]; }
        ok[4] = noh || g_bad(h0, taga, tagb) == 0u;
        if (__all(all && ok[4])) break;
      }
#pragma unroll
      for (int i = 0; i < 2; ++i)
        *(LAS unsigned*)(L + L_Q + wv * 512 + i * 256 + lane * 4) = pack2(g_val(qv[i][0]) + g_val(qv[2 + i][0]), g_val(qv[i][2]) + g_val(qv[2 + i][2]));
      if (isq && lane == 0) { flags[F_TAGA] = taga; flags[F_TAGB] = tagb; flags[F_EP0] = ep[0]; flags[F_EP1] = ep[1]; }
      LDS_INC(F_Q);
    }
    if (!isq) {
      if (lane < 2) {
        const float xb0 = bf2f(x[rbt * DM + rcol]) + bf2f(b2[rcol]), xb1 = bf2f(x[rbt * DM + rcol + 16]) + bf2f(b2[rcol + 16]);
        *(LAS f32x2*)(L + L_XB + lane * 8) = f32x2{xb0, xb1};
      }
      const float a0 = xrow_sum(g_val(h0[0])), a1 = xrow_sum(g_val(h0[2]));
      const unsigned hv = rb < 14 ? pack2(gelu_tanh(rbf(rbf(a0) + bflo(bv))), gelu_tanh(rbf(rbf(a1) + bfhi(bv)))) : 0u;
      if (lane < 16) *(LAS unsigned*)(L + L_H + lane * 4) = hv;
      return;
    }
    LDS_SPIN(VPROG() < 8u);
    {
      float ms = 0.f, ls = 0.f;
      const unsigned mo = WS_ML + (g * 32 + (lane & 31)) * 128 + (rb >> 2) * 16;
      const u64 miss = poll_pair(ws, mo, taga, tagb, ms, ls, 0xffffffffull, H1LEFT);
      flags[F_GO1] = 1u;
      if (miss) poll_pair(ws, mo, taga, tagb, ms, ls, miss, 0u);
      const float e = ex2((ms - row_max(ms)) * LOG2E);
      const float w = e * __builtin_amdgcn_rcpf(row_sum(e * ls));
      if (lane < 32) *(LAS float*)(L + L_W + lane * 4) = w;
      BARRIER();
      poll_pair(ws, WS_H2 + (rch * 128 + (lane & 7) * 16 + (j & 15)) * 4096 + ru * 16, taga, tagb, ms, ls, 0xffull, H2LEFT);
      flags[F_GO2] = 1u;
    }
    return;
  }

  const int sw = wv - NAUX;
  __builtin_amdgcn_s_setprio(2);
  u32x4 kf[4][4];
  {
    const unsigned char* kbase = (const unsigned char*)kc + kvoff;
    const unsigned ko = lr * 256 + lg * 16;
#pragma unroll
    for (int t = 0; t < 4; ++t) {
      if (t == 1) BARRIER();
      asm volatile("global_load_dwordx4 %0, %4, %5\n\tglobal_load_dwordx4 %1, %4, %5 offset:64\n\t"
                   "global_load_dwordx4 %2, %4, %5 offset:128\n\tglobal_load_dwordx4 %3, %4, %5 offset:192"
                   : "=&v"(kf[t][0]), "=&v"(kf[t][1]), "=&v"(kf[t][2]), "=&v"(kf[t][3]) : "v"(ko), "s"(kbase + tile(8 * t + sw)) : "memory");
    }
  }
  LDS_INC(F_KISS);
  LDS_SPIN(flags[F_Q] < 2u);
  u32x4 qf[4];
#pragma unroll
  for (int s = 0; s < 4; ++s) qf[s] = *(LAS u32x4*)(L + L_Q + (lr & 3) * 256 + s * 64 + lg * 16);
  const unsigned taga = flags[F_TAGA], tagb = flags[F_TAGB];
  asm volatile("s_waitcnt vmcnt(0)"
               : "+v"(kf[0][0]), "+v"(kf[0][1]), "+v"(kf[0][2]), "+v"(kf[0][3]), "+v"(kf[1][0]), "+v"(kf[1][1]), "+v"(kf[1][2]), "+v"(kf[1][3]),
                 "+v"(kf[2][0]), "+v"(kf[2][1]), "+v"(kf[2][2]), "+v"(kf[2][3]), "+v"(kf[3][0]), "+v"(kf[3][1]), "+v"(kf[3][2]), "+v"(kf[3][3])
               :: "memory");
  float mcu;
  {
    float sc[4][4];
    float m = -INFINITY;
#pragma unroll
    for (int t = 0; t < 4; ++t) {
      f32x4 a = {0.f, 0.f, 0.f, 0.f};
#pragma unroll
      for (int s = 0; s < 4; ++s) a = mfma32(kf[t][s], qf[s], a);
      const unsigned r01 = pack2(a[0], a[1]), r23 = pack2(a[2], a[3]);
      const unsigned s01 = pack2(bflo(r01) * SCALE, bfhi(r01) * SCALE), s23 = pack2(bflo(r23) * SCALE, bfhi(r23) * SCALE);
      sc[t][0] = bflo(s01); sc[t][1] = bfhi(s01); sc[t][2] = bflo(s23); sc[t][3] = bfhi(s23);
      m = fmaxf(fmaxf(m, sc[t][0]), fmaxf(sc[t][1], fmaxf(sc[t][2], sc[t][3])));
    }
    m = xrow_max(m);
    if (lane < 4) *(LAS float*)(L + L_M + lane * 32 + sw * 4) = m;
    LDS_INC(F_MAX);
    LDS_SPIN_HOT(flags[F_MAX] < 8u);
    {
      const f32x4 m0 = *(LAS f32x4*)(L + L_M + (lr & 3) * 32), m1 = *(LAS f32x4*)(L + L_M + (lr & 3) * 32 + 16);
      m = fmaxf(fmaxf(fmaxf(m0[0], m0[1]), fmaxf(m0[2], m0[3])), fmaxf(fmaxf(m1[0], m1[1]), fmaxf(m1[2], m1[3])));
    }
    mcu = m;
    float l = 0.f;
#pragma unroll
    for (int t = 0; t < 4; ++t) {
#pragma unroll
      for (int r = 0; r < 4; ++r) { sc[t][r] = ex2((sc[t][r] - m) * LOG2E); l += sc[t][r]; }
      if (lr < 4) *(LAS u32x2*)(L + L_P + (4 * t + (sw >> 1)) * 256 + lr * 64 + lg * 16 + (sw & 1) * 8) = u32x2{pack2(sc[t][0], sc[t][1]), pack2(sc[t][2], sc[t][3])};
    }
    l = xrow_sum(l);
    if (lane < 4) *(LAS float*)(L + L_L + lane * 32 + sw * 4) = l;
    LDS_INC(F_P);
  }
  LDS_SPIN_HOT(flags[F_P] < 8u);
  u32x4 gml;
  {
    const f32x4 l0 = *(LAS f32x4*)(L + L_L + (lr & 3) * 32), l1 = *(LAS f32x4*)(L + L_L + (lr & 3) * 32 + 16);
    gml = u32x4{g_lo(mcu, tagb), taga, g_lo(((l0[0] + l0[1]) + (l0[2] + l0[3])) + ((l1[0] + l1[1]) + (l1[2] + l1[3])), tagb), taga};
    asm volatile("" : "+v"(gml));
  }
  f32x4 o = {0.f, 0.f, 0.f, 0.f};
  {
    unsigned vp = 0u;
    u32x4 pf[16];
#pragma unroll
    for (int s = 0; s < 16; ++s) pf[s] = *(LAS u32x4*)(L + L_P + s * 256 + (lr & 3) * 64 + lg * 16);
    const unsigned tb = L_V + (4 * lg + (lr >> 2)) * 256 + ((sw ^ (4 * (lg & 1) + (lr >> 2))) << 5) + 8 * (lr & 3);
#pragma unroll
    for (int s = 0; s < 16; ++s) {
      if (s == 14 && sw == 0 && lane < 4) __builtin_amdgcn_raw_buffer_store_b128(gml, rs, (int)(WS_ML + me * 128) + lane * 16, 0, AUX_SC1);
      if (s == 0) VWAIT(2u);
      if (s == 4) VWAIT(4u);
      if (s == 8) VWAIT(6u);
      if (s == 12) VWAIT(7u);
      if (s == 14) VWAIT(8u);
      const u32x2 lo = tr_read(L, tb + s * 8192), hi = tr_read(L, tb + s * 8192 + 4096);
      o = mfma32(u32x4{lo[0], lo[1], hi[0], hi[1]}, pf[s], o);
      if (s == 3 || s == 7 || s == 11 || s == 13) SCHED();
    }
  }
  if (lr < 8) {
    const float v0 = (lr & 4) ? o[2] : o[0], v1 = (lr & 4) ? o[3] : o[1];
    const u32x4 gv = {g_lo(v0, tagb), taga, g_lo(v1, tagb), taga};
    __builtin_amdgcn_raw_buffer_store_b128(gv, rs, (int)(WS_H1 + me * 4096 + sw * 128) + (lr & 3) * 1024 + lg * 32 + (lr & 4) * 4, 0, AUX_SC1);
  }
  {
    const unsigned oo = WS_H1 + ((g * 32 + sw * 4 + lg) * 4 + (rb >> 2)) * 1024 + (rb & 3) * 256 + lr * 16;
    float v0 = 0.f, v1 = 0.f;
    LDS_SPIN_HOT(flags[F_GO1] == 0u);
    poll_pair(ws, oo, taga, tagb, v0, v1, ~0ull, 0u);
    *(LAS unsigned*)(L + L_OX + (sw * 4 + lg) * 64 + lr * 4) = pack2(v0, v1);
  }
  BARRIER();
  {
    const int q = lr >> 2;
    const unsigned ta = L_WST + (8 * lg + q) * 512 + (((2 * sw) ^ (q | ((lg & 1) << 2))) << 5) + 8 * (lr & 3);
    const unsigned t2 = L_WST + 16384 + (4 * lg + q) * 512 + (((2 * sw) ^ ((4 * lg + q) & 7)) << 5) + 8 * (lr & 3);
    const u32x4 of0 = *(LAS u32x4*)(L + L_OX + lr * 64 + lg * 16), of1 = *(LAS u32x4*)(L + L_OX + 1024 + lr * 64 + lg * 16);
    const f32x4 w0 = *(LAS f32x4*)(L + L_W + lg * 16), w1v = *(LAS f32x4*)(L + L_W + 64 + lg * 16);
    const u32x2 hf = *(LAS u32x2*)(L + L_H + (lr & 1) * 32 + lg * 8);
    float r[2][2];
#pragma unroll
    for (int t = 0; t < 2; ++t) {
      const u32x2 lo = tr_read(L, ta ^ (t << 5)), hi = tr_read(L, (ta ^ (t << 5)) + 2048);
      const u32x2 w2f = tr_read(L, t2 ^ (t << 5));
      const u32x4 wf = {lo[0], lo[1], hi[0], hi[1]};
      const f32x4 a0 = mfma32(of0, wf, f32x4{0.f, 0.f, 0.f, 0.f}), a1 = mfma32(of1, wf, f32x4{0.f, 0.f, 0.f, 0.f});
      const f32x4 mac = mfma16(hf, w2f, f32x4{0.f, 0.f, 0.f, 0.f});
      const float p0 = (a0[0] * w0[0] + a0[1] * w0[1]) + (a0[2] * w0[2] + a0[3] * w0[3]);
      const float p1v = (a1[0] * w1v[0] + a1[1] * w1v[1]) + (a1[2] * w1v[2] + a1[3] * w1v[3]);
      auto sx = __builtin_amdgcn_permlane16_swap(__float_as_uint(p0), __float_as_uint(p1v), false, false);
      r[t][0] = x32_sum(__uint_as_float(sx[0]) + __uint_as_float(sx[1]));
      r[t][1] = (lg & 1) ? mac[1] : mac[0];
    }
    if (lane < 32) {
      const u32x4 gv = {g_lo(r[0][0] + r[0][1], tagb), taga, g_lo(r[1][0] + r[1][1], tagb), taga};
      __builtin_amdgcn_raw_buffer_store_b128(gv, rs, (int)(WS_H2 + ((ch * 128 + g * 16 + rb) * 8 + sw) * 512) + lane * 16, 0, AUX_SC1);
    }
  }
  if (sw == 0) {
    const unsigned ho = WS_H2 + (rch * 128 + (lane >> 1) * 4) * 4096 + ru * 16;
    const f32x2 xb = *(LAS f32x2*)(L + L_XB + (lane & 1) * 8);
    float s0 = 0.f, s1 = 0.f;
    LDS_SPIN_HOT(flags[F_GO2] == 0u);
    poll_quad(ws, ho, taga, tagb, s0, s1);
    s0 += dpp<XOR2>(s0); s1 += dpp<XOR2>(s1);
    s0 += dpp<ROR4>(s0); s1 += dpp<ROR4>(s1);
    s0 += dpp<ROR8>(s0); s1 += dpp<ROR8>(s1);
    s0 = xrow_sum(s0); s1 = xrow_sum(s1);
    if (lane < 2) {
      out[rbt * DM + rcol] = (u16)f2bf(xb[0] + s0);
      out[rbt * DM + rcol + 16] = (u16)f2bf(xb[1] + s1);
    }
    if (me == 0 && lane == 0) {
      const u64 e = (((u64)flags[F_EP1] << 32) | flags[F_EP0]) + 1ull;
      __hip_atomic_store((GAS u64*)(ws + WS_EPOCH), e, __ATOMIC_RELAXED, __HIP_MEMORY_SCOPE_AGENT);
    }
  }
}

extern "C" void kernel_launch(void* const* d_in, const int* in_sizes, int n_in, void* d_out, int out_size, void* d_ws, size_t ws_size,
                              hipStream_t stream) {
  (void)hipFuncSetAttribute((const void*)fused_kernel, hipFuncAttributeMaxDynamicSharedMemorySize, L_BYTES);
  fused_kernel<<<256, NTHR, L_BYTES, stream>>>((const u16*)d_in[0], (const u16*)d_in[1], (const u16*)d_in[2], (const u16*)d_in[3],
                                               (const u16*)d_in[4], (const u16*)d_in[6], (const u16*)d_in[7], (const u16*)d_in[8],
                                               (const u16*)d_in[9], (u16*)d_out, (unsigned char*)d_ws);
}
```

```cpp
#include <hip/hip_runtime.h>
#include <hip/hip_bf16.h>
#include <stdint.h>

#define GAS __attribute__((address_space(1)))
#define LAS __attribute__((address_space(3)))
typedef unsigned u32x4 __attribute__((ext_vector_type(4)));
typedef unsigned u32x2 __attribute__((ext_vector_type(2)));
typedef float f32x4 __attribute__((ext_vector_type(4)));
typedef float f32x2 __attribute__((ext_vector_type(2)));
typedef short s16x4 __attribute__((ext_vector_type(4)));
typedef __bf16 bf16x8 __attribute__((ext_vector_type(8)));
typedef unsigned short u16;
typedef unsigned long long u64;

constexpr int DM = 512, HQ = 32, HKV = 8, SK = 8192, DH = 128, FF = 1792;
constexpr int NAUX = 2, NSTR = 8, NLD = 4, NTHR = (NAUX + NSTR + NLD) * 64;
constexpr float SCALE = 0.08838834764831845f, LOG2E = 1.4426950408889634f;
constexpr unsigned SPIN_CAP = 1u << 17;
constexpr int TG = 8, TSKEW = 32;
constexpr int TQ = 480, TV0 = 490, DV = 63, TW = 998;
constexpr int QSLEEP = 12;
constexpr unsigned H1LEFT = 3, H2LEFT = 1;

constexpr unsigned WS_EPOCH = 0;
constexpr unsigned WS_ML = 393216;
constexpr unsigned WS_Q = 32768;
constexpr unsigned WS_HP = WS_Q + 131072;
constexpr unsigned WS_H1 = 524288;
constexpr unsigned WS_H2 = WS_H1 + (1u << 20);
constexpr unsigned WS_END = WS_H2 + (1u << 20);

constexpr unsigned L_V = 0;
constexpr unsigned L_WST = 131072;
constexpr unsigned L_XN = L_WST + 24576;
constexpr unsigned L_P = L_XN;
constexpr unsigned L_Q = L_XN + 4096;
constexpr unsigned L_OX = L_Q + 1024;
constexpr unsigned L_M = L_OX + 2048;
constexpr unsigned L_L = L_M + 128;
constexpr unsigned L_W = L_L + 128;
constexpr unsigned L_H = L_W + 128;
constexpr unsigned L_XB = L_H + 64;
constexpr unsigned L_FLAG = L_XB + 32;
constexpr unsigned L_BYTES = L_FLAG + 64;
static_assert(L_BYTES <= 160 * 1024, "LDS");
enum { F_VP = 0, F_Q = 4, F_MAX = 5, F_P = 6, F_GO1 = 14, F_GO2 = 15, F_WIN = 13, F_TAGA = 8, F_TAGB = 9, F_EP0 = 10, F_EP1 = 11 };

__device__ __forceinline__ float bf2f(unsigned b) { return __builtin_bit_cast(float, b << 16); }
__device__ __forceinline__ float bflo(unsigned u) { return __builtin_bit_cast(float, u << 16); }
__device__ __forceinline__ float bfhi(unsigned u) { return __builtin_bit_cast(float, u & 0xffff0000u); }
__device__ __forceinline__ unsigned f2bf(float f) { return (unsigned)__builtin_bit_cast(u16, (__bf16)f); }
__device__ __forceinline__ float rbf(float f) { return bf2f(f2bf(f)); }
__device__ __forceinline__ unsigned pack2(float lo, float hi) {
  typedef __bf16 bf16x2 __attribute__((ext_vector_type(2)));
  const f32x2 v = {lo, hi};
  return __builtin_bit_cast(unsigned, __builtin_convertvector(v, bf16x2));
}
__device__ __forceinline__ f32x4 mfma32(u32x4 a, u32x4 b, f32x4 c) {
  return __builtin_amdgcn_mfma_f32_16x16x32_bf16(__builtin_bit_cast(bf16x8, a), __builtin_bit_cast(bf16x8, b), c, 0, 0, 0);
}
__device__ __forceinline__ f32x4 mfma16(u32x2 a, u32x2 b, f32x4 c) {
  return __builtin_amdgcn_mfma_f32_16x16x16bf16_1k(__builtin_bit_cast(s16x4, a), __builtin_bit_cast(s16x4, b), c, 0, 0, 0);
}
template <int CTRL> __device__ __forceinline__ float dpp(float x) {
  return __builtin_bit_cast(float, __builtin_amdgcn_mov_dpp(__builtin_bit_cast(int, x), CTRL, 0xf, 0xf, true));
}
constexpr int XOR1 = 0xB1, XOR2 = 0x4E, HMIR = 0x141, MIR = 0x140, ROR4 = 0x124, ROR8 = 0x128;
__device__ __forceinline__ float row_sum(float v) { v += dpp<XOR1>(v); v += dpp<XOR2>(v); v += dpp<HMIR>(v); v += dpp<MIR>(v); return v; }
__device__ __forceinline__ float row_max(float v) {
  v = fmaxf(v, dpp<XOR1>(v)); v = fmaxf(v, dpp<XOR2>(v)); v = fmaxf(v, dpp<HMIR>(v)); v = fmaxf(v, dpp<MIR>(v)); return v;
}
__device__ __forceinline__ float x16_sum(float x) {
  auto s = __builtin_amdgcn_permlane16_swap(__float_as_uint(x), __float_as_uint(x), false, false);
  return __uint_as_float(s[0]) + __uint_as_float(s[1]);
}
__device__ __forceinline__ float x32_sum(float x) {
  auto t = __builtin_amdgcn_permlane32_swap(__float_as_uint(x), __float_as_uint(x), false, false);
  return __uint_as_float(t[0]) + __uint_as_float(t[1]);
}
__device__ __forceinline__ float xrow_sum(float x) { return x32_sum(x16_sum(x)); }
__device__ __forceinline__ float xrow_max(float x) {
  auto s = __builtin_amdgcn_permlane16_swap(__float_as_uint(x), __float_as_uint(x), false, false);
  x = fmaxf(__uint_as_float(s[0]), __uint_as_float(s[1]));
  auto t = __builtin_amdgcn_permlane32_swap(__float_as_uint(x), __float_as_uint(x), false, false);
  return fmaxf(__uint_as_float(t[0]), __uint_as_float(t[1]));
}
__device__ __forceinline__ float ex2(float x) { return __builtin_amdgcn_exp2f(x); }

#define CBAR() asm volatile("" ::: "memory")
#define SCHED() __builtin_amdgcn_sched_barrier(0)
#define WAIT_VM(n) asm volatile("s_waitcnt vmcnt(" #n ")" ::: "memory")
#define BARRIER() do { asm volatile("s_waitcnt lgkmcnt(0)" ::: "memory"); __builtin_amdgcn_s_barrier(); CBAR(); } while (0)
#define LDS_SPIN(cond) do { _Pragma("nounroll") for (unsigned spins_ = 0; spins_ < (SPIN_CAP << 4) && (cond); ++spins_) __builtin_amdgcn_s_sleep(1); CBAR(); } while (0)
#define LDS_SPIN_HOT(cond) do { _Pragma("nounroll") for (unsigned spins_ = 0; spins_ < (SPIN_CAP << 5) && (cond); ++spins_) {} CBAR(); } while (0)
#define VPROG() vprog(*(volatile LAS u32x4*)(L + L_FLAG))
#define VWAIT(n) do { _Pragma("nounroll") for (unsigned spins_ = 0; spins_ < (SPIN_CAP << 5) && vp < (n); ++spins_) vp = __builtin_amdgcn_readfirstlane(VPROG()); CBAR(); } while (0)
#define LDS_INC(i) do { CBAR(); if (lane == 0) __hip_atomic_fetch_add((LAS unsigned*)(L + L_FLAG + 4 * (i)), 1u, __ATOMIC_RELAXED, __HIP_MEMORY_SCOPE_WORKGROUP); CBAR(); } while (0)

__device__ __forceinline__ void dma4_kv(const void* sbase, unsigned v0, unsigned v1, unsigned v2, unsigned v3, unsigned dst) {
  unsigned keep;
  asm volatile(
      "s_mov_b32 %0, m0\n\ts_mov_b32 m0, %6\n\ts_nop 0\n\t"
      "global_load_lds_dwordx4 %1, %5\n\t"
      "global_load_lds_dwordx4 %2, %5 offset:1024\n\t"
      "global_load_lds_dwordx4 %3, %5 offset:2048\n\t"
      "global_load_lds_dwordx4 %4, %5 offset:3072\n\t"
      "s_mov_b32 m0, %0"
      : "=&s"(keep) : "v"(v0), "v"(v1), "v"(v2), "v"(v3), "s"(sbase), "s"(dst) : "memory");
}
#define DMA_STEP "global_load_lds_dwordx4 %1, %2\n\ts_add_u32 m0, m0, 0x400\n\tv_add_u32 %1, %4, %1\n\t"
template <int N> __device__ __forceinline__ void dma_w(const void* sbase, unsigned voff, unsigned step, unsigned dst) {
  unsigned keep;
  if constexpr (N == 4)
    asm volatile("s_mov_b32 %0, m0\n\ts_mov_b32 m0, %3\n\ts_nop 0\n\t" DMA_STEP DMA_STEP DMA_STEP DMA_STEP "s_mov_b32 m0, %0"
                 : "=&s"(keep), "+v"(voff) : "s"(sbase), "s"(dst), "s"(step) : "memory", "scc");
  else
    asm volatile("s_mov_b32 %0, m0\n\ts_mov_b32 m0, %3\n\ts_nop 0\n\t" DMA_STEP DMA_STEP "s_mov_b32 m0, %0"
                 : "=&s"(keep), "+v"(voff) : "s"(sbase), "s"(dst), "s"(step) : "memory", "scc");
}
__device__ __forceinline__ void dma1_w(const void* sbase, unsigned voff, unsigned dst) {
  unsigned keep;
  asm volatile("s_mov_b32 %0, m0\n\ts_mov_b32 m0, %3\n\ts_nop 0\n\tglobal_load_lds_dwordx4 %1, %2\n\ts_mov_b32 m0, %0"
               : "=&s"(keep) : "v"(voff), "s"(sbase), "s"(dst) : "memory");
}
__device__ __forceinline__ u32x2 tr_read(LAS unsigned char* L, unsigned a) {
  return __builtin_bit_cast(u32x2, __builtin_amdgcn_ds_read_tr16_b64_v4i16((LAS s16x4*)(L + a)));
}
__device__ __forceinline__ unsigned g_lo(float v, unsigned tagb) {
  unsigned u = __builtin_bit_cast(unsigned, v);
  u += 0x7fu + ((u >> 8) & 1u);
  return (u & 0xffffff00u) | tagb;
}
__device__ __forceinline__ float g_val(unsigned lo) { return __builtin_bit_cast(float, lo & 0xffffff00u); }
__device__ __forceinline__ unsigned g_bad(u32x4 v, unsigned taga, unsigned tagb) {
  return (v[1] ^ taga) | (v[3] ^ taga) | (((v[0] ^ tagb) | (v[2] ^ tagb)) & 0xffu);
}
__device__ __forceinline__ unsigned vprog(u32x4 v) { return min(min(v[0], v[1]), min(v[2], v[3])); }
constexpr int AUX_SC1 = 16;

__device__ __forceinline__ u64 poll_pair(const void* sbase, unsigned voff, unsigned taga, unsigned tagb, float& r0, float& r1, u64 pend, unsigned left) {
  unsigned t0, t1, t2, cnt, nb;
  u64 sv;
  asm volatile(
      "s_mov_b64 %[sv], exec\n\ts_mov_b32 %[cn], 0x8000\n\t"
      "1:\n\t"
      "s_mov_b64 exec, %[pe]\n\t"
      "global_load_dwordx4 v[100:103], %[vo], %[sb] sc1\n\t"
      "s_waitcnt vmcnt(0)\n\t"
      "v_xor_b32 %[t0], v101, %[ta]\n\tv_xor_b32 %[t1], v100, %[tb]\n\tv_xor_b32 %[t2], v102, %[tb]\n\tv_or_b32 %[t1], %[t1], %[t2]\n\t"
      "v_xor_b32 %[t2], v103, %[ta]\n\tv_and_b32 %[t1], 0xff, %[t1]\n\tv_or3_b32 %[t0], %[t0], %[t1], %[t2]\n\t"
      "v_cmp_eq_u32 vcc, 0, %[t0]\n\t"
      "s_mov_b64 exec, vcc\n\t"
      "v_and_b32 %[r0], 0xffffff00, v100\n\tv_and_b32 %[r1], 0xffffff00, v102\n\t"
      "s_andn2_b64 %[pe], %[pe], vcc\n\t"
      "s_bcnt1_i32_b64 %[nb], %[pe]\n\t"
      "s_cmp_le_u32 %[nb], %[lf]\n\t"
      "s_cbranch_scc1 2f\n\t"
      "s_sub_u32 %[cn], %[cn], 1\n\ts_cbranch_scc0 1b\n\t"
      "2:\n\ts_mov_b64 exec, %[sv]\n\t"
      : [r0] "+v"(r0), [r1] "+v"(r1), [t0] "=&v"(t0), [t1] "=&v"(t1), [t2] "=&v"(t2), [sv] "=&s"(sv), [pe] "+s"(pend), [cn] "=&s"(cnt), [nb] "=&s"(nb)
      : [vo] "v"(voff), [sb] "s"(sbase), [ta] "v"(taga), [tb] "v"(tagb), [lf] "s"(left)
      : "memory", "vcc", "scc", "v100", "v101", "v102", "v103");
  return pend;
}

#define QLOAD "global_load_dwordx4 v[100:103], %[vo], %[sb] sc1\n\tglobal_load_dwordx4 v[104:107], %[v1], %[sb] sc1\n\t" \
              "global_load_dwordx4 v[108:111], %[v2], %[sb] sc1\n\tglobal_load_dwordx4 v[112:115], %[v3], %[sb] sc1\n\t"
__device__ __forceinline__ void poll_quad(const void* sbase, unsigned voff, unsigned taga, unsigned tagb, float& r0, float& r1) {
  unsigned t0, t1, t2;
  u64 sv, pend;
  unsigned cnt;
  asm volatile(
      "s_mov_b64 %[sv], exec\n\ts_mov_b64 %[pe], exec\n\ts_mov_b32 %[cn], 0x8000\n\t"
      "1:\n\t" QLOAD
      "s_waitcnt vmcnt(0)\n\t"
      "v_xor_b32 %[t0], v101, %[ta]\n\tv_xor_b32 %[t1], v100, %[tb]\n\t"
      "v_bitop3_b32 %[t0], v103, %[ta], %[t0] bitop3:0xbe\n\tv_bitop3_b32 %[t1], v102, %[tb], %[t1] bitop3:0xbe\n\t"
      "v_bitop3_b32 %[t0], v105, %[ta], %[t0] bitop3:0xbe\n\tv_bitop3_b32 %[t1], v104, %[tb], %[t1] bitop3:0xbe\n\t"
      "v_bitop3_b32 %[t0], v107, %[ta], %[t0] bitop3:0xbe\n\tv_bitop3_b32 %[t1], v106, %[tb], %[t1] bitop3:0xbe\n\t"
      "v_bitop3_b32 %[t0], v109, %[ta], %[t0] bitop3:0xbe\n\tv_bitop3_b32 %[t1], v108, %[tb], %[t1] bitop3:0xbe\n\t"
      "v_bitop3_b32 %[t0], v111, %[ta], %[t0] bitop3:0xbe\n\tv_bitop3_b32 %[t1], v110, %[tb], %[t1] bitop3:0xbe\n\t"
      "v_bitop3_b32 %[t0], v113, %[ta], %[t0] bitop3:0xbe\n\tv_bitop3_b32 %[t1], v112, %[tb], %[t1] bitop3:0xbe\n\t"
      "v_bitop3_b32 %[t0], v115, %[ta], %[t0] bitop3:0xbe\n\tv_bitop3_b32 %[t1], v114, %[tb], %[t1] bitop3:0xbe\n\t"
      "v_and_b32 %[t1], 0xff, %[t1]\n\t"
      "v_or_b32 %[t0], %[t0], %[t1]\n\t"
      "v_cmp_eq_u32 vcc, 0, %[t0]\n\t"
      "s_mov_b64 exec, vcc\n\t"
      "v_and_b32 v100, 0xffffff00, v100\n\tv_and_b32 v102, 0xffffff00, v102\n\tv_and_b32 v104, 0xffffff00, v104\n\tv_and_b32 v106, 0xffffff00, v106\n\t"
      "v_and_b32 v108, 0xffffff00, v108\n\tv_and_b32 v110, 0xffffff00, v110\n\tv_and_b32 v112, 0xffffff00, v112\n\tv_and_b32 v114, 0xffffff00, v114\n\t"
      "v_add_f32 v100, v100, v104\n\tv_add_f32 v102, v102, v106\n\tv_add_f32 v108, v108, v112\n\tv_add_f32 v110, v110, v114\n\t"
      "v_add_f32 %[r0], v100, v108\n\tv_add_f32 %[r1], v102, v110\n\t"
      "s_andn2_b64 %[pe], %[pe], vcc\n\t"
      "s_cbranch_scc0 2f\n\t"
      "s_mov_b64 exec, %[pe]\n\t"
      "s_sub_u32 %[cn], %[cn], 1\n\ts_cbranch_scc0 1b\n\t"
      "2:\n\ts_mov_b64 exec, %[sv]\n\t"
      : [r0] "+v"(r0), [r1] "+v"(r1), [t0] "=&v"(t0), [t1] "=&v"(t1), [t2] "=&v"(t2), [sv] "=&s"(sv), [pe] "=&s"(pend), [cn] "=&s"(cnt)
      : [vo] "v"(voff), [v1] "v"(voff + 4096u), [v2] "v"(voff + 8192u), [v3] "v"(voff + 12288u), [sb] "s"(sbase), [ta] "v"(taga), [tb] "v"(tagb)
      : "memory", "vcc", "scc", "v100", "v101", "v102", "v103", "v104", "v105", "v106", "v107", "v108", "v109", "v110", "v111", "v112", "v113", "v114", "v115");
}


__device__ __forceinline__ float gelu_tanh(float x) {
  const float u = 0.7978845608028654f * (x + 0.044715f * x * x * x);
  const float e = ex2(u * (2.0f * LOG2E));
  const float t = 1.0f - 2.0f * __builtin_amdgcn_rcpf(e + 1.0f);
  return 0.5f * x * (1.0f + t);
}

__global__ void __launch_bounds__(NTHR) __attribute__((amdgpu_num_vgpr(100)))
fused_kernel(const u16* __restrict__ x, const u16* __restrict__ kc, const u16* __restrict__ vc, const u16* __restrict__ wq,
             const u16* __restrict__ wo, const u16* __restrict__ w1, const u16* __restrict__ b1, const u16* __restrict__ w2,
             const u16* __restrict__ b2, u16* __restrict__ out, unsigned char* __restrict__ ws) {
  extern __shared__ __attribute__((aligned(16))) unsigned char smem[];
  LAS unsigned char* L = (LAS unsigned char*)smem;
  const unsigned lbase = (unsigned)(size_t)L;
  volatile LAS unsigned* flags = (volatile LAS unsigned*)(L + L_FLAG);
  const int tid = threadIdx.x, lane = tid & 63, wv = __builtin_amdgcn_readfirstlane(tid >> 6);
  const int g = blockIdx.x & 7, j = blockIdx.x >> 3;
  const int b = j >> 4, sp = j & 15, rb = j >> 1, ch = j & 1;
  const int lg = lane >> 4, lr = lane & 15;
  const int me = g * 32 + j;
  const int rch = me >> 7, ru = (me & 127) * 2 + (lane & 1);
  const int rcol = rch * 256 + (ru >> 5) * 32 + (ru & 15), rbt = (ru >> 4) & 1;
  const bool hasw1 = (j >> 2) != 3;
  const int p1 = hasw1 ? (j < 12 ? j : j - 4) * 8 + g : 0;
  const size_t kvoff = (size_t)(b * HKV + g) * SK * (DH * 2);
  auto tile = [&](int idx) { return (unsigned)((((idx / TG) * 16 + ((sp + idx / TG) & 15)) * TG + idx % TG + TSKEW * (b * HKV + g)) & 511) * 4096u; };
  const __amdgpu_buffer_rsrc_t rs = __builtin_amdgcn_make_buffer_rsrc((void*)ws, 0, (int)WS_END, 0x00020000);
  asm volatile("" ::"s"(x), "s"(kc), "s"(vc), "s"(wq), "s"(wo), "s"(w1), "s"(b1), "s"(w2), "s"(b2), "s"(out), "s"(ws));

  if (wv >= NAUX + NSTR) {
    const int ld = wv - (NAUX + NSTR);
    volatile LAS unsigned* prog = flags + F_VP + ld;
    const u64 tstart = __builtin_amdgcn_s_memrealtime();
    __builtin_amdgcn_s_setprio(3);
    dma_w<4>((const unsigned char*)(wq + (size_t)((j & 1) * 256 + ld * 64) * (HQ * DH) + g * 512 + (j >> 1) * 32), (lane >> 2) * (HQ * DH * 2) + (lane & 3) * 16,
             16 * HQ * DH * 2, lbase + L_WST + ld * 4096);
    if (hasw1)
      dma_w<2>((const unsigned char*)(w1 + (size_t)((p1 & 3) * 128 + ld * 32) * FF + (p1 >> 2) * 32), (lane >> 2) * (FF * 2) + (lane & 3) * 16, 16 * FF * 2,
               lbase + L_WST + 16384 + ld * 2048);
    __builtin_amdgcn_s_setprio(0);
    BARRIER();
    WAIT_VM(0);
    LDS_INC(F_WIN);
    const unsigned char* vbase = (const unsigned char*)vc + kvoff;
    const unsigned dst = lbase + L_V + ld * 4096;
    const unsigned ve = lg * 256 + ((((lr >> 1) ^ lg) << 1) | (lr & 1)) * 16, vo = ve ^ 128u;
    _Pragma("nounroll") for (int c = 0; c < 8; ++c) {
      while (__builtin_amdgcn_s_memrealtime() - tstart < (u64)(TV0 + DV * c)) __builtin_amdgcn_s_sleep(1);
      dma4_kv(vbase + tile(4 * c + ld), ve, vo, ve, vo, dst + c * 16384);
      if (c == 5 || c == 7) { WAIT_VM(16); *prog = (unsigned)(c - 3); }
    }
    {
      LDS_SPIN(flags[F_Q] < 2u);
      while (__builtin_amdgcn_s_memrealtime() - tstart < (u64)TW) __builtin_amdgcn_s_sleep(1);
      const int hl = lane >> 5, slot = lane & 31, rb2 = rb < 14 ? rb : 0;
      const unsigned char* wob = (const unsigned char*)(wo + (size_t)(g * 512 + rb * 32 + ld * 8) * DM + ch * 256);
      const unsigned char* w2b = (const unsigned char*)(w2 + (size_t)(g * 224 + rb2 * 16 + ld * 4) * DM + ch * 256);
      const unsigned base = hl * 1024 + slot * 16;
      _Pragma("nounroll") for (int i = 0; i < 4; ++i)
        dma1_w(wob + i * 2048, base ^ ((((ld & 1) << 2) | ((i & 1) << 1) | hl) << 5), lbase + L_WST + ld * 4096 + i * 1024);
      _Pragma("nounroll") for (int i = 0; i < 2; ++i)
        dma1_w(w2b + i * 2048, base ^ (((4 * ld + 2 * i + hl) & 7) << 5), lbase + L_WST + 16384 + ld * 2048 + i * 1024);
    }
    WAIT_VM(14); *prog = 6u;
    WAIT_VM(10); *prog = 7u;
    WAIT_VM(6); *prog = 8u;
    WAIT_VM(0);
    return;
  }

  if (wv < 2) {
    __builtin_amdgcn_s_setprio(3);
    const bool isq = (wv == 0);
    const u64 tstart = __builtin_amdgcn_s_memrealtime();
    const int rb2 = rb < 14 ? rb : 0;
    u32x4 xr0, xr1;
    u32x2 ep;
    unsigned bv;
    {
      const unsigned xo = lane * 32, zo = 0u, bo = (unsigned)((g * 224 + rb2 * 16 + 2 * (lane & 7)) * 2);
      asm volatile("global_load_dwordx4 %0, %4, %7\n\tglobal_load_dwordx4 %1, %4, %7 offset:16\n\t"
                   "global_load_dwordx2 %2, %5, %8 sc1\n\tglobal_load_dword %3, %6, %9"
                   : "=&v"(xr0), "=&v"(xr1), "=&v"(ep), "=&v"(bv) : "v"(xo), "v"(zo), "v"(bo), "s"(x), "s"(ws), "s"(b1) : "memory");
    }
    const int k0 = isq ? (j & 1) * 256 : (p1 & 3) * 128;
    const unsigned stg = isq ? L_WST : L_WST + 16384;
    if (isq && lane < 16) flags[lane] = 0u;
    BARRIER();
    WAIT_VM(0);
    asm volatile("; PIN x" : "+v"(xr0), "+v"(xr1), "+v"(ep), "+v"(bv)::"memory");
    {
      float f[16];
#pragma unroll
      for (int i = 0; i < 4; ++i) { f[2 * i] = bflo(xr0[i]); f[2 * i + 1] = bfhi(xr0[i]); f[8 + 2 * i] = bflo(xr1[i]); f[9 + 2 * i] = bfhi(xr1[i]); }
      float s = 0.f;
#pragma unroll
      for (int i = 0; i < 16; ++i) s += f[i];
      const float mean = x16_sum(row_sum(s)) * (1.0f / DM);
      float q = 0.f;
#pragma unroll
      for (int i = 0; i < 16; ++i) { const float d = f[i] - mean; q += d * d; }
      const float var = x16_sum(row_sum(q)) * (1.0f / DM);
      const float mb = rbf(mean);
      const float inv = __builtin_amdgcn_rcpf(rbf(__builtin_amdgcn_sqrtf(rbf(rbf(var) + 1e-5f))));
      u32x4 o0, o1;
#pragma unroll
      for (int i = 0; i < 4; ++i) {
        const unsigned d0 = pack2(f[2 * i] - mb, f[2 * i + 1] - mb), d1 = pack2(f[8 + 2 * i] - mb, f[9 + 2 * i] - mb);
        o0[i] = pack2(bflo(d0) * inv, bfhi(d0) * inv);
        o1[i] = pack2(bflo(d1) * inv, bfhi(d1) * inv);
      }
      *(LAS u32x4*)(L + L_XN + wv * 2048 + lane * 32) = o0;
      *(LAS u32x4*)(L + L_XN + wv * 2048 + lane * 32 + 16) = o1;
    }
    const unsigned taga = ep[0] + 1u, tagb = (ep[1] + (taga == 0u ? 1u : 0u)) & 0xffu;
    f32x4 acc[2] = {f32x4{0.f, 0.f, 0.f, 0.f}, f32x4{0.f, 0.f, 0.f, 0.f}};
    {
      unsigned ta = stg + (8 * lg + (lr >> 2)) * 64 + 8 * (lr & 3);
      unsigned xa = L_XN + wv * 2048 + (lr & 1) * 1024 + k0 * 2 + lg * 16;
      LDS_SPIN(flags[F_WIN] < 4u);
      _Pragma("nounroll") for (int hb = 0; hb < 2; ++hb) {
#pragma unroll
        for (int s = 0; s < 4; ++s) {
          const u32x4 xf = *(LAS u32x4*)(L + xa + s * 64);
#pragma unroll
          for (int t = 0; t < 2; ++t) {
            const u32x2 lo = tr_read(L, ta + s * 2048 + t * 32), hi = tr_read(L, ta + s * 2048 + t * 32 + 256);
            acc[t] = mfma32(u32x4{lo[0], lo[1], hi[0], hi[1]}, xf, acc[t]);
          }
        }
        if (!isq) break;
        ta += 8192; xa += 256;
      }
    }
    if (lr < 8 && (isq || hasw1)) {
      const f32x4 a = (lr & 4) ? acc[1] : acc[0];
      const float v0 = (lr & 2) ? a[2] : a[0], v1 = (lr & 2) ? a[3] : a[1];
      const u32x4 gv = {g_lo(v0, tagb), taga, g_lo(v1, tagb), taga};
      const unsigned pb = isq ? WS_Q + (g * 2 + (j & 1)) * 4096 + (j >> 1) * 256 : WS_HP + (p1 >> 2) * 2048 + (p1 & 3) * 256;
      const unsigned sb = isq ? 65536u : 128u, st = isq ? 128u : 1024u;
      __builtin_amdgcn_raw_buffer_store_b128(gv, rs, (int)(pb + (lr & 1) * sb + (lr >> 2) * st + lg * 32 + (lr & 2) * 8), 0, AUX_SC1);
    }
    __builtin_amdgcn_s_sleep(QSLEEP);
    while (__builtin_amdgcn_s_memrealtime() - tstart < (u64)TQ) __builtin_amdgcn_s_sleep(1);
    u32x4 h0 = {0u, 0u, 0u, 0u};
    {
      const int qo = (int)(WS_Q + (b * 8 + g) * 8192) + wv * 2048 + lane * 16;
      const int ho = (int)(WS_HP + (g * 14 + rb2) * 1024) + lane * 16;
      u32x4 qv[4];
      bool ok[5];
#pragma unroll
      for (int i = 0; i < 4; ++i) { qv[i] = u32x4{0u, 0u, 0u, 0u}; ok[i] = false; }
      const bool noh = isq || rb >= 14;
      ok[4] = noh;
      _Pragma("nounroll") for (unsigned spins = 0; spins < (SPIN_CAP >> 2); ++spins) {
        CBAR();
#pragma unroll
        for (int i = 0; i < 4; ++i) if (!ok[i]) qv[i] = __builtin_amdgcn_raw_buffer_load_b128(rs, qo + (i & 1) * 1024 + (i >> 1) * 4096, 0, AUX_SC1);
        if (!ok[4]) h0 = __builtin_amdgcn_raw_buffer_load_b128(rs, ho, 0, AUX_SC1);
        bool all = true;
#pragma unroll
        for (int i = 0; i < 4; ++i) { ok[i] = g_bad(qv[i], taga, tagb) == 0u; all = all && ok[i]; }
        ok[4] = noh || g_bad(h0, taga, tagb) == 0u;
        if (__all(all && ok[4])) break;
      }
#pragma unroll
      for (int i = 0; i < 2; ++i)
        *(LAS unsigned*)(L + L_Q + wv * 512 + i * 256 + lane * 4) = pack2(g_val(qv[i][0]) + g_val(qv[2 + i][0]), g_val(qv[i][2]) + g_val(qv[2 + i][2]));
      if (isq && lane == 0) { flags[F_TAGA] = taga; flags[F_TAGB] = tagb; flags[F_EP0] = ep[0]; flags[F_EP1] = ep[1]; }
      LDS_INC(F_Q);
    }
    if (!isq) {
      if (lane < 2) {
        const float xb0 = bf2f(x[rbt * DM + rcol]) + bf2f(b2[rcol]), xb1 = bf2f(x[rbt * DM + rcol + 16]) + bf2f(b2[rcol + 16]);
        *(LAS f32x2*)(L + L_XB + lane * 8) = f32x2{xb0, xb1};
      }
      const float a0 = xrow_sum(g_val(h0[0])), a1 = xrow_sum(g_val(h0[2]));
      const unsigned hv = rb < 14 ? pack2(gelu_tanh(rbf(rbf(a0) + bflo(bv))), gelu_tanh(rbf(rbf(a1) + bfhi(bv)))) : 0u;
      if (lane < 16) *(LAS unsigned*)(L + L_H + lane * 4) = hv;
      return;
    }
    LDS_SPIN(VPROG() < 8u);
    {
      float ms = 0.f, ls = 0.f;
      const unsigned mo = WS_ML + (g * 32 + (lane & 31)) * 128 + (rb >> 2) * 16;
      const u64 miss = poll_pair(ws, mo, taga, tagb, ms, ls, 0xffffffffull, H1LEFT);
      flags[F_GO1] = 1u;
      if (miss) poll_pair(ws, mo, taga, tagb, ms, ls, miss, 0u);
      const float e = ex2((ms - row_max(ms)) * LOG2E);
      const float w = e * __builtin_amdgcn_rcpf(row_sum(e * ls));
      if (lane < 32) *(LAS float*)(L + L_W + lane * 4) = w;
      BARRIER();
      poll_pair(ws, WS_H2 + (rch * 128 + (lane & 7) * 16 + (j & 15)) * 4096 + ru * 16, taga, tagb, ms, ls, 0xffull, H2LEFT);
      flags[F_GO2] = 1u;
    }
    return;
  }

  const int sw = wv - NAUX;
  __builtin_amdgcn_s_setprio(2);
  u32x4 kf[4][4];
  {
    const unsigned char* kbase = (const unsigned char*)kc + kvoff;
    const unsigned ko = lr * 256 + lg * 16;
#pragma unroll
    for (int t = 0; t < 4; ++t) {
      if (t == 1) BARRIER();
      asm volatile("global_load_dwordx4 %0, %4, %5\n\tglobal_load_dwordx4 %1, %4, %5 offset:64\n\t"
                   "global_load_dwordx4 %2, %4, %5 offset:128\n\tglobal_load_dwordx4 %3, %4, %5 offset:192"
                   : "=&v"(kf[t][0]), "=&v"(kf[t][1]), "=&v"(kf[t][2]), "=&v"(kf[t][3]) : "v"(ko), "s"(kbase + tile(8 * t + sw)) : "memory");
    }
  }
  LDS_SPIN(flags[F_Q] < 2u);
  u32x4 qf[4];
#pragma unroll
  for (int s = 0; s < 4; ++s) qf[s] = *(LAS u32x4*)(L + L_Q + (lr & 3) * 256 + s * 64 + lg * 16);
  const unsigned taga = flags[F_TAGA], tagb = flags[F_TAGB];
  asm volatile("s_waitcnt vmcnt(0)"
               : "+v"(kf[0][0]), "+v"(kf[0][1]), "+v"(kf[0][2]), "+v"(kf[0][3]), "+v"(kf[1][0]), "+v"(kf[1][1]), "+v"(kf[1][2]), "+v"(kf[1][3]),
                 "+v"(kf[2][0]), "+v"(kf[2][1]), "+v"(kf[2][2]), "+v"(kf[2][3]), "+v"(kf[3][0]), "+v"(kf[3][1]), "+v"(kf[3][2]), "+v"(kf[3][3])
               :: "memory");
  float mcu;
  {
    float sc[4][4];
    float m = -INFINITY;
#pragma unroll
    for (int t = 0; t < 4; ++t) {
      f32x4 a = {0.f, 0.f, 0.f, 0.f};
#pragma unroll
      for (int s = 0; s < 4; ++s) a = mfma32(kf[t][s], qf[s], a);
      const unsigned r01 = pack2(a[0], a[1]), r23 = pack2(a[2], a[3]);
      const unsigned s01 = pack2(bflo(r01) * SCALE, bfhi(r01) * SCALE), s23 = pack2(bflo(r23) * SCALE, bfhi(r23) * SCALE);
      sc[t][0] = bflo(s01); sc[t][1] = bfhi(s01); sc[t][2] = bflo(s23); sc[t][3] = bfhi(s23);
      m = fmaxf(fmaxf(m, sc[t][0]), fmaxf(sc[t][1], fmaxf(sc[t][2], sc[t][3])));
    }
    m = xrow_max(m);
    if (lane < 4) *(LAS float*)(L + L_M + lane * 32 + sw * 4) = m;
    LDS_INC(F_MAX);
    LDS_SPIN_HOT(flags[F_MAX] < 8u);
    {
      const f32x4 m0 = *(LAS f32x4*)(L + L_M + (lr & 3) * 32), m1 = *(LAS f32x4*)(L + L_M + (lr & 3) * 32 + 16);
      m = fmaxf(fmaxf(fmaxf(m0[0], m0[1]), fmaxf(m0[2], m0[3])), fmaxf(fmaxf(m1[0], m1[1]), fmaxf(m1[2], m1[3])));
    }
    mcu = m;
    float l = 0.f;
#pragma unroll
    for (int t = 0; t < 4; ++t) {
#pragma unroll
      for (int r = 0; r < 4; ++r) { sc[t][r] = ex2((sc[t][r] - m) * LOG2E); l += sc[t][r]; }
      if (lr < 4) *(LAS u32x2*)(L + L_P + (4 * t + (sw >> 1)) * 256 + lr * 64 + lg * 16 + (sw & 1) * 8) = u32x2{pack2(sc[t][0], sc[t][1]), pack2(sc[t][2], sc[t][3])};
    }
    l = xrow_sum(l);
    if (lane < 4) *(LAS float*)(L + L_L + lane * 32 + sw * 4) = l;
    LDS_INC(F_P);
  }
  LDS_SPIN_HOT(flags[F_P] < 8u);
  u32x4 gml;
  {
    const f32x4 l0 = *(LAS f32x4*)(L + L_L + (lr & 3) * 32), l1 = *(LAS f32x4*)(L + L_L + (lr & 3) * 32 + 16);
    gml = u32x4{g_lo(mcu, tagb), taga, g_lo(((l0[0] + l0[1]) + (l0[2] + l0[3])) + ((l1[0] + l1[1]) + (l1[2] + l1[3])), tagb), taga};
    asm volatile("" : "+v"(gml));
  }
  f32x4 o = {0.f, 0.f, 0.f, 0.f};
  {
    unsigned vp = 0u;
    u32x4 pf[16];
#pragma unroll
    for (int s = 0; s < 16; ++s) pf[s] = *(LAS u32x4*)(L + L_P + s * 256 + (lr & 3) * 64 + lg * 16);
    const unsigned tb = L_V + (4 * lg + (lr >> 2)) * 256 + ((sw ^ (4 * (lg & 1) + (lr >> 2))) << 5) + 8 * (lr & 3);
#pragma unroll
    for (int s = 0; s < 16; ++s) {
      if (s == 14 && sw == 0 && lane < 4) __builtin_amdgcn_raw_buffer_store_b128(gml, rs, (int)(WS_ML + me * 128) + lane * 16, 0, AUX_SC1);
      if (s == 0) VWAIT(2u);
      if (s == 4) VWAIT(4u);
      if (s == 8) VWAIT(6u);
      if (s == 12) VWAIT(7u);
      if (s == 14) VWAIT(8u);
      const u32x2 lo = tr_read(L, tb + s * 8192), hi = tr_read(L, tb + s * 8192 + 4096);
      o = mfma32(u32x4{lo[0], lo[1], hi[0], hi[1]}, pf[s], o);
      if (s == 3 || s == 7 || s == 11 || s == 13) SCHED();
    }
  }
  if (lr < 8) {
    const float v0 = (lr & 4) ? o[2] : o[0], v1 = (lr & 4) ? o[3] : o[1];
    const u32x4 gv = {g_lo(v0, tagb), taga, g_lo(v1, tagb), taga};
    __builtin_amdgcn_raw_buffer_store_b128(gv, rs, (int)(WS_H1 + me * 4096 + sw * 128) + (lr & 3) * 1024 + lg * 32 + (lr & 4) * 4, 0, AUX_SC1);
  }
  {
    const unsigned oo = WS_H1 + ((g * 32 + sw * 4 + lg) * 4 + (rb >> 2)) * 1024 + (rb & 3) * 256 + lr * 16;
    float v0 = 0.f, v1 = 0.f;
    LDS_SPIN_HOT(flags[F_GO1] == 0u);
    poll_pair(ws, oo, taga, tagb, v0, v1, ~0ull, 0u);
    *(LAS unsigned*)(L + L_OX + (sw * 4 + lg) * 64 + lr * 4) = pack2(v0, v1);
  }
  BARRIER();
  {
    const int q = lr >> 2;
    const unsigned ta = L_WST + (8 * lg + q) * 512 + (((2 * sw) ^ (q | ((lg & 1) << 2))) << 5) + 8 * (lr & 3);
    const unsigned t2 = L_WST + 16384 + (4 * lg + q) * 512 + (((2 * sw) ^ ((4 * lg + q) & 7)) << 5) + 8 * (lr & 3);
    const u32x4 of0 = *(LAS u32x4*)(L + L_OX + lr * 64 + lg * 16), of1 = *(LAS u32x4*)(L + L_OX + 1024 + lr * 64 + lg * 16);
    const f32x4 w0 = *(LAS f32x4*)(L + L_W + lg * 16), w1v = *(LAS f32x4*)(L + L_W + 64 + lg * 16);
    const u32x2 hf = *(LAS u32x2*)(L + L_H + (lr & 1) * 32 + lg * 8);
    float r[2][2];
#pragma unroll
    for (int t = 0; t < 2; ++t) {
      const u32x2 lo = tr_read(L, ta ^ (t << 5)), hi = tr_read(L, (ta ^ (t << 5)) + 2048);
      const u32x2 w2f = tr_read(L, t2 ^ (t << 5));
      const u32x4 wf = {lo[0], lo[1], hi[0], hi[1]};
      const f32x4 a0 = mfma32(of0, wf, f32x4{0.f, 0.f, 0.f, 0.f}), a1 = mfma32(of1, wf, f32x4{0.f, 0.f, 0.f, 0.f});
      const f32x4 mac = mfma16(hf, w2f, f32x4{0.f, 0.f, 0.f, 0.f});
      const float p0 = (a0[0] * w0[0] + a0[1] * w0[1]) + (a0[2] * w0[2] + a0[3] * w0[3]);
      const float p1v = (a1[0] * w1v[0] + a1[1] * w1v[1]) + (a1[2] * w1v[2] + a1[3] * w1v[3]);
      auto sx = __builtin_amdgcn_permlane16_swap(__float_as_uint(p0), __float_as_uint(p1v), false, false);
      r[t][0] = x32_sum(__uint_as_float(sx[0]) + __uint_as_float(sx[1]));
      r[t][1] = (lg & 1) ? mac[1] : mac[0];
    }
    if (lane < 32) {
      const u32x4 gv = {g_lo(r[0][0] + r[0][1], tagb), taga, g_lo(r[1][0] + r[1][1], tagb), taga};
      __builtin_amdgcn_raw_buffer_store_b128(gv, rs, (int)(WS_H2 + ((ch * 128 + g * 16 + rb) * 8 + sw) * 512) + lane * 16, 0, AUX_SC1);
    }
  }
  if (sw == 0) {
    const unsigned ho = WS_H2 + (rch * 128 + (lane >> 1) * 4) * 4096 + ru * 16;
    const f32x2 xb = *(LAS f32x2*)(L + L_XB + (lane & 1) * 8);
    float s0 = 0.f, s1 = 0.f;
    LDS_SPIN_HOT(flags[F_GO2] == 0u);
    poll_quad(ws, ho, taga, tagb, s0, s1);
    s0 += dpp<XOR2>(s0); s1 += dpp<XOR2>(s1);
    s0 += dpp<ROR4>(s0); s1 += dpp<ROR4>(s1);
    s0 += dpp<ROR8>(s0); s1 += dpp<ROR8>(s1);
    s0 = xrow_sum(s0); s1 = xrow_sum(s1);
    if (lane < 2) {
      out[rbt * DM + rcol] = (u16)f2bf(xb[0] + s0);
      out[rbt * DM + rcol + 16] = (u16)f2bf(xb[1] + s1);
    }
    if (me == 0 && lane == 0) {
      const u64 e = (((u64)flags[F_EP1] << 32) | flags[F_EP0]) + 1ull;
      __hip_atomic_store((GAS u64*)(ws + WS_EPOCH), e, __ATOMIC_RELAXED, __HIP_MEMORY_SCOPE_AGENT);
    }
  }
}

extern "C" void kernel_launch(void* const* d_in, const int* in_sizes, int n_in, void* d_out, int out_size, void* d_ws, size_t ws_size,
                              hipStream_t stream) {
  (void)hipFuncSetAttribute((const void*)fused_kernel, hipFuncAttributeMaxDynamicSharedMemorySize, L_BYTES);
  fused_kernel<<<256, NTHR, L_BYTES, stream>>>((const u16*)d_in[0], (const u16*)d_in[1], (const u16*)d_in[2], (const u16*)d_in[3],
                                               (const u16*)d_in[4], (const u16*)d_in[6], (const u16*)d_in[7], (const u16*)d_in[8],
                                               (const u16*)d_in[9], (u16*)d_out, (unsigned char*)d_ws);
}
```
